```python
import torch

def _softmax(x, axis=-1):
    return torch.softmax(x, dim=axis)
def _gelu(x):
    return torch.nn.functional.gelu(x, approximate="tanh")
def _silu(x):
    return torch.nn.functional.silu(x)
def _sigmoid(x):
    return torch.sigmoid(x)
def _layernorm(x, axis=-1, eps=1e-5):
    m = x.mean(dim=axis, keepdim=True)
    v = x.var(dim=axis, keepdim=True, unbiased=False)
    return (x - m) / torch.sqrt(v + eps)
def _scatter_add(tgt, idx, src, axis=0):
    return tgt.clone().index_add_(axis, idx, src)
def _segment_sum(src, seg, n):
    out = torch.zeros((n,) + src.shape[1:], dtype=src.dtype)
    return out.index_add_(0, seg, src)
def _pad(x, pw):
    flat = tuple(v for pair in reversed(pw) for v in pair)
    return torch.nn.functional.pad(x, flat)

kernel_name = 'sequential_preLN__ssm_conv_h3__moe_arch_mod__prompt'

def setup_inputs():

    _w = (lambda a: (lambda t: t.to(torch.bfloat16) if t.is_floating_point() else t)(torch.as_tensor(a)))
    def _A_setup():
        B, S, D = 1, 2048, 1024
        ks = 4
        return {
            "q": _w((lambda *s: torch.randn(*s, dtype=torch.bfloat16) * 0.05)(B, S, D)),
            "k": _w((lambda *s: torch.randn(*s, dtype=torch.bfloat16) * 0.4)(B, S, D)),
            "v": _w((lambda *s: torch.randn(*s, dtype=torch.bfloat16) * 0.4)(B, S, D)),
            "shift_k": _w((lambda *s: torch.randn(*s, dtype=torch.bfloat16) * 0.4)(D, ks) * 0.5),
            "ssm_k": _w((lambda *s: torch.randn(*s, dtype=torch.bfloat16) * 0.4)(D, S) * (1.0 / S ** 0.5)),
            "ssm_D": _w((lambda *s: torch.randn(*s, dtype=torch.bfloat16) * 0.4)(D)),
        }
    _A_in = _A_setup()

    def _B_setup():
        B, S, D, F, C = 1, 2048, 1024, 3584, 1
        return {
            "x": _w((lambda *s: torch.randn(*s, dtype=torch.bfloat16) * 0.05)(B, S, D)),
            "w_r": _w((lambda *s: torch.randn(*s, dtype=torch.bfloat16) * 0.05)(D)),
            "w_gate": _w((lambda *s: torch.randn(*s, dtype=torch.bfloat16) * 0.05)(D, F)),
            "w_up": _w((lambda *s: torch.randn(*s, dtype=torch.bfloat16) * 0.05)(D, F)),
            "w_down": _w((lambda *s: torch.randn(*s, dtype=torch.bfloat16) * 0.05)(F, D)),
        }
    _B_in = _B_setup()

    return {
        "A_q": _A_in["q"],
        "A_k": _A_in["k"],
        "A_v": _A_in["v"],
        "A_shift_k": _A_in["shift_k"],
        "A_ssm_k": _A_in["ssm_k"],
        "A_ssm_D": _A_in["ssm_D"],
        "B_x": _B_in["x"],
        "B_w_r": _B_in["w_r"],
        "B_w_gate": _B_in["w_gate"],
        "B_w_up": _B_in["w_up"],
        "B_w_down": _B_in["w_down"],
    }

def reference(A_q, A_k, A_v, A_shift_k, A_ssm_k, A_ssm_D, B_x, B_w_r, B_w_gate, B_w_up, B_w_down):
    def _A_ref(q, k, v, shift_k, ssm_k, ssm_D):
        B, S, D = q.shape
        ks = shift_k.shape[-1]
        k_pad = _pad(k, ((0, 0), (ks - 1, 0), (0, 0)))
        k_shift = torch.zeros_like(k)
        for t in range(ks):
            k_shift = k_shift + k_pad[:, t:t + S, :] * shift_k[:, ks - 1 - t][None, None, :]
        kv = k_shift * v
        L = 2 * S
        kv_pad = _pad(kv, ((0, 0), (0, S), (0, 0)))
        kf_pad = _pad(ssm_k, ((0, 0), (0, S)))
        kv_f = (lambda x, axes=(-2,-1): torch.fft.rfft2(x.float(), dim=axes))(kv_pad[:, None, :, :], axes=(1, 2))
        kf_f = (lambda x, axes=(-2,-1): torch.fft.rfft2(x.float(), dim=axes))(kf_pad[:, None, :], axes=(1, 2))
        kf_f = (lambda x, *d: x.permute(*d))(kf_f, 1, 2, 0)
        y_f = kv_f * kf_f[None, :, :, :]
        y = (lambda x, s, axes=(-2,-1): torch.fft.irfft2(x, s=s, dim=axes).to(torch.bfloat16))(y_f, (1, L), axes=(1, 2))[:, 0, :S, :]
        y = y + kv * ssm_D[None, None, :]
        return q * y

    def _B_ref(x, w_r, w_gate, w_up, w_down):
        B, S, D = x.shape
        C = 1
        r = x @ w_r
        idx = torch.argsort(-r, axis=-1)[:, :C]
        idx_e = (lambda x, n, axis: torch.repeat_interleave(x, n, dim=axis))(idx.reshape(B, C, 1), D, axis=2)
        x_sel = (lambda x, i, axis: torch.take_along_dim(x, i, dim=axis))(x, idx_e, axis=1)
        g = _silu(x_sel @ w_gate)
        u = x_sel @ w_up
        y = (g * u) @ w_down
        r_sel = _sigmoid((lambda x, i, axis: torch.take_along_dim(x, i, dim=axis))(r, idx, axis=1))
        y = y * r_sel.reshape(B, C, 1)
        b_off = (torch.arange(B) * S).reshape(B, 1)
        flat_idx = (idx + b_off).reshape(B * C)
        zero = (x * 0.0).reshape(B * S, D)
        upd = _scatter_add(zero, flat_idx, y.reshape(B * C, D), axis=0)
        return x + upd.reshape(B, S, D)

    x = A_q
    h = x + _A_ref(_layernorm(x, axis=-1), A_k, A_v, A_shift_k, A_ssm_k, A_ssm_D)
    h2 = h + _B_ref(_layernorm(h, axis=-1), B_w_r, B_w_gate, B_w_up, B_w_down)
    return h2
```

```cpp
#include <hip/hip_runtime.h>
#include <hip/hip_bf16.h>
typedef unsigned short u16;
constexpr int S=2048, D=1024, F=3584;
__device__ __forceinline__ float b2f(u16 x){ return __uint_as_float((unsigned)x<<16); }
__device__ __forceinline__ u16 f2b(float f){ __bf16 b=(__bf16)f; return __builtin_bit_cast(u16,b); }
__device__ __forceinline__ float rb(float f){ return b2f(f2b(f)); }

__global__ void k_kv(const u16* k,const u16* v,const u16* sk,u16* kvT){
  int idx=blockIdx.x*256+threadIdx.x; int s=idx/D, d=idx%D;
  float a=0; for(int j=0;j<4;j++){ if(s-j>=0) a+=b2f(k[(s-j)*D+d])*b2f(sk[d*4+j]); }
  kvT[d*S+s]=f2b(a*b2f(v[idx]));
}
__global__ void k_conv(const u16* kvT,const u16* hk,const u16* Dv,u16* yT){
  __shared__ float x[S], h[S];
  int d=blockIdx.x;
  for(int i=threadIdx.x;i<S;i+=256){ x[i]=b2f(kvT[d*S+i]); h[i]=b2f(hk[d*S+i]); }
  __syncthreads();
  for(int s=threadIdx.x;s<S;s+=256){ float a=0; for(int t=0;t<=s;t++) a+=h[s-t]*x[t];
    yT[d*S+s]=f2b(rb(a)+rb(x[s]*b2f(Dv[d]))); }
}
__device__ float blk_sum(float v,float* sm){
  for(int o=32;o;o>>=1) v+=__shfl_xor(v,o);
  __syncthreads(); if((threadIdx.x&63)==0) sm[threadIdx.x>>6]=v; __syncthreads();
  return sm[0]+sm[1]+sm[2]+sm[3];
}
__global__ void k_rows(const u16* q,const u16* yT,const u16* wr,u16* out,u16* hbuf,u16* hnbuf,float* r){
  __shared__ float sm[4];
  int s=blockIdx.x; float x[4],h[4],hn[4];
  for(int i=0;i<4;i++) x[i]=b2f(q[s*D+threadIdx.x*4+i]);
  float m=blk_sum(x[0]+x[1]+x[2]+x[3],sm)/D;
  float vv=0; for(int i=0;i<4;i++) vv+=(x[i]-m)*(x[i]-m);
  vv=blk_sum(vv,sm)/D;
  float mb=rb(m), db=rb(sqrtf(rb(rb(vv)+1e-5f)));
  for(int i=0;i<4;i++){ float qn=rb(rb(x[i]-mb)/db); h[i]=rb(x[i]+rb(qn*b2f(yT[(threadIdx.x*4+i)*S+s]))); }
  m=blk_sum(h[0]+h[1]+h[2]+h[3],sm)/D;
  vv=0; for(int i=0;i<4;i++) vv+=(h[i]-m)*(h[i]-m);
  vv=blk_sum(vv,sm)/D;
  mb=rb(m); db=rb(sqrtf(rb(rb(vv)+1e-5f)));
  float rr=0;
  for(int i=0;i<4;i++){ hn[i]=rb(rb(h[i]-mb)/db); rr+=hn[i]*b2f(wr[threadIdx.x*4+i]);
    int o=s*D+threadIdx.x*4+i; out[o]=f2b(h[i]+hn[i]); hbuf[o]=f2b(h[i]); hnbuf[o]=f2b(hn[i]); }
  rr=blk_sum(rr,sm); if(threadIdx.x==0) r[s]=rb(rr);
}
__device__ int find_idx(const float* r){
  __shared__ float bv[256]; __shared__ int bi[256];
  float b=-INFINITY; int ix=0; for(int i=threadIdx.x;i<S;i+=256){ if(r[i]>b){b=r[i];ix=i;} }
  bv[threadIdx.x]=b; bi[threadIdx.x]=ix; __syncthreads();
  for(int o=128;o;o>>=1){ if(threadIdx.x<o){ if(bv[threadIdx.x+o]>bv[threadIdx.x] || (bv[threadIdx.x+o]==bv[threadIdx.x] && bi[threadIdx.x+o]<bi[threadIdx.x])){bv[threadIdx.x]=bv[threadIdx.x+o];bi[threadIdx.x]=bi[threadIdx.x+o];} } __syncthreads(); }
  return bi[0];
}
__global__ void k_gu(const float* r,const u16* hn,const u16* wg,const u16* wu,float* act){
  int ix=find_idx(r); int f=blockIdx.x*256+threadIdx.x; float g=0,u=0;
  for(int d=0;d<D;d++){ float x=b2f(hn[ix*D+d]); g+=x*b2f(wg[d*F+f]); u+=x*b2f(wu[d*F+f]); }
  g=rb(g); u=rb(u); g=rb(g/(1.f+__expf(-g))); act[f]=rb(g*u);
}
__global__ void k_down(const float* r,const u16* h,const u16* hn,const float* act,const u16* wd,u16* out){
  int ix=find_idx(r); int d=blockIdx.x*256+threadIdx.x; float y=0;
  for(int f=0;f<F;f++) y+=act[f]*b2f(wd[f*D+d]);
  y=rb(y); float sg=rb(1.f/(1.f+__expf(-r[ix]))); y=rb(y*sg);
  out[ix*D+d]=f2b(b2f(h[ix*D+d])+rb(b2f(hn[ix*D+d])+y));
}
extern "C" void kernel_launch(void* const* d_in, const int* in_sizes, int n_in, void* d_out, int out_size, void* d_ws, size_t ws_size, hipStream_t stream){
  const u16 *q=(const u16*)d_in[0],*k=(const u16*)d_in[1],*v=(const u16*)d_in[2],*sk=(const u16*)d_in[3],*hk=(const u16*)d_in[4],*Dv=(const u16*)d_in[5],
    *wr=(const u16*)d_in[7],*wg=(const u16*)d_in[8],*wu=(const u16*)d_in[9],*wd=(const u16*)d_in[10];
  char* ws=(char*)d_ws; u16* kvT=(u16*)ws; u16* yT=(u16*)(ws+(4<<20)); u16* hb=(u16*)(ws+(8<<20)); u16* hnb=(u16*)(ws+(12<<20)); float* r=(float*)(ws+(16<<20)); float* act=(float*)(ws+(17<<20));
  u16* out=(u16*)d_out;
  k_kv<<<S*D/256,256,0,stream>>>(k,v,sk,kvT);
  k_conv<<<D,256,0,stream>>>(kvT,hk,Dv,yT);
  k_rows<<<S,256,0,stream>>>(q,yT,wr,out,hb,hnb,r);
  k_gu<<<F/256,256,0,stream>>>(r,hnb,wg,wu,act);
  k_down<<<D/256,256,0,stream>>>(r,hb,hnb,act,wd,out);
}
```

```cpp
#include <hip/hip_runtime.h>
#include <hip/hip_bf16.h>

#define GAS __attribute__((address_space(1)))
#define LAS __attribute__((address_space(3)))
typedef unsigned short u16;
typedef unsigned long long u64;
typedef unsigned v4u __attribute__((ext_vector_type(4)));
typedef unsigned v2u __attribute__((ext_vector_type(2)));
typedef float f32x4 __attribute__((ext_vector_type(4)));
typedef float f32x2 __attribute__((ext_vector_type(2)));
typedef float f32x8 __attribute__((ext_vector_type(8)));
typedef __bf16 bf16x2 __attribute__((ext_vector_type(2)));
typedef __bf16 bf16x8 __attribute__((ext_vector_type(8)));
typedef short s16x4 __attribute__((ext_vector_type(4)));
typedef __amdgpu_buffer_rsrc_t rsrc_t;

constexpr int SEQ = 2048, DM = 1024, FF = 3584;
constexpr int NWG = 256, NTHR = 512, NFS = 224;
constexpr unsigned SENT = 0xFFFFFFFFu;
constexpr int SC1 = 16;

constexpr int OFF_EPOCH = 0;
constexpr int OFF_FL = 4096;
constexpr int OFF_CAND = 16384;
constexpr int OFF_ROWS = 32768;
constexpr int OFF_PART = OFF_ROWS + (1 << 20);
constexpr int OFF_X1 = 4 << 20;
constexpr int OFF_X2 = 8 << 20;
constexpr int OFF_DBG = 16 << 20;
constexpr int WS_USED = 32 << 20;

constexpr int HC_COPY = 260 * 16;
constexpr int HC_CH = 8 * HC_COPY;
constexpr int XB_OFF = 4 * HC_CH;
constexpr int XB_CH = 286 * 16;
constexpr int MISC = XB_OFF + 4 * XB_CH;
constexpr int L_SYNC = MISC;
constexpr int L_ST1 = MISC + 256;
constexpr int L_RED = MISC + 512;
constexpr int L_GU = MISC + 1024;
constexpr int L_TR = MISC + 2048;
constexpr int LDS_BYTES = L_TR + 4096;
constexpr int W_CLR = 0, W_OK = 4, W_HC = 8, W_CB = 12, W_HB = 16, W_WIN = 20;

constexpr unsigned SPIN_G = 1u << 18, SPIN_L = 1u << 22;

#define STAMP(i) do {} while (0)

struct Params {
    const u16 *q, *k, *v, *sk, *hk, *dv, *wr, *wg, *wu, *wd;
    u16* out;
    unsigned char* ws;
};

__device__ __forceinline__ rsrc_t mk(const void* p, unsigned bytes) { return __builtin_amdgcn_make_buffer_rsrc((void*)p, 0, bytes, 0x00020000); }
__device__ __forceinline__ float lo(unsigned x) { return __uint_as_float(x << 16); }
__device__ __forceinline__ float hi(unsigned x) { return __uint_as_float(x & 0xFFFF0000u); }
__device__ __forceinline__ unsigned pk(float a, float b) { f32x2 t = {a, b}; return __builtin_bit_cast(unsigned, __builtin_convertvector(t, bf16x2)); }
__device__ __forceinline__ float rb(float a) { return lo(pk(a, a) & 0xFFFFu); }
__device__ __forceinline__ unsigned nosent(unsigned x) { return x < 0xFFFFFFFEu ? x : 0xFFFFFFFEu; }
__device__ __forceinline__ unsigned umax(unsigned a, unsigned b) { return a > b ? a : b; }
__device__ __forceinline__ bool valid4(v4u x) { return umax(umax(x.x, x.y), umax(x.z, x.w)) != SENT; }
__device__ __forceinline__ f32x4 mfma(v4u a, v4u b, f32x4 c) {
    return __builtin_amdgcn_mfma_f32_16x16x32_bf16(__builtin_bit_cast(bf16x8, a), __builtin_bit_cast(bf16x8, b), c, 0, 0, 0);
}
template <int CTRL> __device__ __forceinline__ float dppf(float x) {
    return __uint_as_float(__builtin_amdgcn_update_dpp(0u, __float_as_uint(x), CTRL, 0xF, 0xF, true));
}
template <int CTRL> __device__ __forceinline__ unsigned dppu(unsigned x) { return __builtin_amdgcn_update_dpp(0u, x, CTRL, 0xF, 0xF, true); }
__device__ __forceinline__ float row_sum(float x) {
    x += dppf<0x128>(x); x += dppf<0x124>(x); x += dppf<0x122>(x); x += dppf<0x121>(x);
    return x;
}
__device__ __forceinline__ float fold32(float a, float b) {
    auto r = __builtin_amdgcn_permlane32_swap(__float_as_uint(a), __float_as_uint(b), false, false);
    return __uint_as_float(r[0]) + __uint_as_float(r[1]);
}
__device__ __forceinline__ float fold16(float a, float b) {
    auto r = __builtin_amdgcn_permlane16_swap(__float_as_uint(a), __float_as_uint(b), false, false);
    return __uint_as_float(r[0]) + __uint_as_float(r[1]);
}
__device__ __forceinline__ float wave_sum(float x) { x = fold32(x, x); x = fold16(x, x); return row_sum(x); }
__device__ __forceinline__ float wave_max(float x) {
    auto r = __builtin_amdgcn_permlane32_swap(__float_as_uint(x), __float_as_uint(x), false, false);
    x = fmaxf(__uint_as_float(r[0]), __uint_as_float(r[1]));
    auto s = __builtin_amdgcn_permlane16_swap(__float_as_uint(x), __float_as_uint(x), false, false);
    x = fmaxf(__uint_as_float(s[0]), __uint_as_float(s[1]));
    x = fmaxf(x, dppf<0x128>(x)); x = fmaxf(x, dppf<0x124>(x)); x = fmaxf(x, dppf<0x122>(x)); x = fmaxf(x, dppf<0x121>(x));
    return x;
}
__device__ __forceinline__ float rdl(float x, int l) { return __uint_as_float(__builtin_amdgcn_readlane(__float_as_uint(x), l)); }

#define CFENCE() asm volatile("" ::: "memory")
__device__ __forceinline__ void lds_post(LAS unsigned char* L, int word, unsigned val) {
    CFENCE();
    *(volatile LAS unsigned*)(L + L_SYNC + 4 * word) = val;
    CFENCE();
}
template <bool SLEEP> __device__ __forceinline__ void lds_wait4(LAS unsigned char* L, int group, unsigned val) {
#pragma nounroll
    for (unsigned s = 0; s < SPIN_L; ++s) {
        const v4u x = *(volatile LAS v4u*)(L + L_SYNC + 4 * group);
        const unsigned m = __builtin_amdgcn_readfirstlane(min(min(x.x, x.y), min(x.z, x.w)));
        if (m >= val) break;
        if (SLEEP) __builtin_amdgcn_s_sleep(1);
    }
    CFENCE();
}
template <bool SLEEP> __device__ __forceinline__ unsigned lds_wait1(LAS unsigned char* L, int word) {
    unsigned m = 0;
#pragma nounroll
    for (unsigned s = 0; s < SPIN_L; ++s) {
        m = __builtin_amdgcn_readfirstlane(*(volatile LAS unsigned*)(L + L_SYNC + 4 * word));
        if (m != 0) break;
        if (SLEEP) __builtin_amdgcn_s_sleep(1);
    }
    CFENCE();
    return m;
}
template <int N> __device__ __forceinline__ void poll16(rsrc_t rs, const int (&off)[N], v4u (&x)[N]) {
#pragma nounroll
    for (unsigned s = 0; s < SPIN_G; ++s) {
        bool ok = true;
#pragma unroll
        for (int i = 0; i < N; ++i) x[i] = __builtin_amdgcn_raw_buffer_load_b128(rs, off[i], 0, SC1);
#pragma unroll
        for (int i = 0; i < N; ++i) ok &= valid4(x[i]);
        if (ok) break;
        CFENCE();
    }
    CFENCE();
}

__global__ void __launch_bounds__(NTHR, 2) h3_mod_kernel(Params p) {
    extern __shared__ __attribute__((aligned(16))) unsigned char smem[];
    LAS unsigned char* L = (LAS unsigned char*)smem;
    const int tid = threadIdx.x, lane = tid & 63;
    const int wave = __builtin_amdgcn_readfirstlane(tid >> 6), w = wave & 3;
    const int b = blockIdx.x, G = ((b & 7) << 5) | (b >> 3);
    const rsrc_t ws = mk(p.ws, WS_USED);
    const int tg = G >> 3, cg = G & 7;
    const int quad = lane >> 3, rg = lane & 7;
    const int ch0 = 128 * cg + 32 * w + 4 * quad;
    const int chc = 16 * (G >> 2) + (G & 3) + 4 * w;
    const int t4 = 64 * w + lane;
    STAMP(0);

    if (wave < 4) {
        __builtin_amdgcn_s_setprio(2);
        const rsrc_t rk = mk(p.k, SEQ * DM * 2), rv = mk(p.v, SEQ * DM * 2), rq = mk(p.q, SEQ * DM * 2);
        const int row0 = 64 * tg + 8 * rg;
        const int vo = row0 * 2048 + ch0 * 2;
        const int vh = row0 >= 3 ? vo - 3 * 2048 : (int)0x80000000;
        v2u kr[11], vr[8], qr[8];
#pragma unroll
        for (int i = 0; i < 3; ++i) kr[i] = __builtin_amdgcn_raw_buffer_load_b64(rk, vh, i * 2048, 0);
        const v4u sa = *(const GAS v4u*)(p.sk + ch0 * 4), sb = *(const GAS v4u*)(p.sk + ch0 * 4 + 8);
#pragma unroll
        for (int i = 0; i < 8; ++i) {
            kr[3 + i] = __builtin_amdgcn_raw_buffer_load_b64(rk, vo, i * 2048, 0);
            vr[i] = __builtin_amdgcn_raw_buffer_load_b64(rv, vo, i * 2048, 0);
        }
        if (lane < 30) *(LAS v4u*)(L + XB_OFF + w * XB_CH + lane * 16) = (v4u){0u, 0u, 0u, 0u};
        asm volatile("s_waitcnt lgkmcnt(0)" ::: "memory");
        __builtin_amdgcn_s_barrier();
        CFENCE();
        {
            const f32x2 w01[4] = {{lo(sa.x), lo(sa.z)}, {hi(sa.x), hi(sa.z)}, {lo(sa.y), lo(sa.w)}, {hi(sa.y), hi(sa.w)}};
            const f32x2 w23[4] = {{lo(sb.x), lo(sb.z)}, {hi(sb.x), hi(sb.z)}, {lo(sb.y), lo(sb.w)}, {hi(sb.y), hi(sb.w)}};
            f32x2 ka[11], kb[11], xa[8], xb[8];
#pragma unroll
            for (int i = 0; i < 11; ++i) { ka[i] = (f32x2){lo(kr[i].x), hi(kr[i].x)}; kb[i] = (f32x2){lo(kr[i].y), hi(kr[i].y)}; }
#pragma unroll
            for (int t = 0; t < 8; ++t) {
                f32x2 a = ka[t] * w01[3], c = kb[t] * w23[3];
#pragma unroll
                for (int j = 2; j >= 0; --j) { a = __builtin_elementwise_fma(ka[t + 3 - j], w01[j], a); c = __builtin_elementwise_fma(kb[t + 3 - j], w23[j], c); }
                xa[t] = a * (f32x2){lo(vr[t].x), hi(vr[t].x)};
                xb[t] = c * (f32x2){lo(vr[t].y), hi(vr[t].y)};
            }
            v4u o[4];
#pragma unroll
            for (int m = 0; m < 4; ++m) {
                o[0][m] = nosent(pk(xa[7 - 2 * m].x, xa[6 - 2 * m].x));
                o[1][m] = nosent(pk(xa[7 - 2 * m].y, xa[6 - 2 * m].y));
                o[2][m] = nosent(pk(xb[7 - 2 * m].x, xb[6 - 2 * m].x));
                o[3][m] = nosent(pk(xb[7 - 2 * m].y, xb[6 - 2 * m].y));
            }
            STAMP(1);
            lds_wait1<false>(L, W_CLR + w);
#pragma unroll
            for (int c = 0; c < 4; ++c)
                __builtin_amdgcn_raw_buffer_store_b128(o[c], ws, OFF_X1 + (ch0 + c) * 4096 + ((tg + ch0 + c) & 31) * 128 + rg * 16, 0, SC1);
        }
        STAMP(2);
#pragma unroll
        for (int i = 0; i < 8; ++i) qr[i] = __builtin_amdgcn_raw_buffer_load_b64(rq, G * 16384 + t4 * 8, i * 2048, 0);
        const v2u wrr = *(const GAS v2u*)(p.wr + t4 * 4);
        v4u aD;
        {
            const unsigned dbits = p.dv[chc];
            const int i = lane & 15, g = lane >> 4, j = 7 - (i & 7);
            const unsigned val = (g == 2 + (i >> 3)) ? (dbits << (16 * (j & 1))) : 0u;
            aD = (v4u){(j >> 1) == 0 ? val : 0u, (j >> 1) == 1 ? val : 0u, (j >> 1) == 2 ? val : 0u, (j >> 1) == 3 ? val : 0u};
        }
        lds_wait4<false>(L, W_OK, 1);
        STAMP(3);
        {
            int off[4]; v4u x[4];
#pragma unroll
            for (int i = 0; i < 4; ++i) off[i] = OFF_X1 + chc * 4096 + ((8 * i + quad + chc) & 31) * 128 + rg * 16;
            poll16<4>(ws, off, x);
#pragma unroll
            for (int i = 0; i < 4; ++i) *(LAS v4u*)(L + XB_OFF + w * XB_CH + (30 + 64 * i + lane) * 16) = x[i];
        }
        STAMP(4);
        lds_wait1<false>(L, W_HC + w);
        STAMP(5);
        f32x4 acc[8];
        {
            const int i = lane & 15, g = lane >> 4;
            const LAS unsigned char* ap = L + w * HC_CH + (i & 7) * HC_COPY + (3 - g + (i >> 3)) * 16;
            const LAS unsigned char* bp = L + XB_OFF + w * XB_CH + (28 + 2 * i + g) * 16;
            v4u A0[8], B0[8], A1[8], B1[8];
#pragma unroll
            for (int u = 0; u < 8; ++u) B0[u] = *(const LAS v4u*)(bp + 512 * u);
#pragma unroll
            for (int m = 0; m < 8; ++m) A0[m] = *(const LAS v4u*)(ap + 512 * m);
#pragma unroll
            for (int n = 0; n < 8; ++n) acc[n] = mfma(aD, B0[n], (f32x4){0.f, 0.f, 0.f, 0.f});
#pragma unroll 1
            for (int r2 = 0; r2 < 4; ++r2) {
#pragma unroll
                for (int u = 0; u < 8; ++u) { B1[u] = *(const LAS v4u*)(bp - 64 + 512 * u); A1[u] = *(const LAS v4u*)(ap + 64 + 512 * u); }
#pragma unroll
                for (int m = 0; m < 8; ++m)
#pragma unroll
                    for (int n = m; n < 8; ++n) acc[n] = mfma(A0[m], B0[n - m], acc[n]);
                ap += 128; bp -= 128;
                if (r2 < 3) {
#pragma unroll
                    for (int u = 0; u < 8; ++u) { B0[u] = *(const LAS v4u*)(bp + 512 * u); A0[u] = *(const LAS v4u*)(ap + 512 * u); }
                }
#pragma unroll
                for (int m = 0; m < 8; ++m)
#pragma unroll
                    for (int n = m; n < 8; ++n) acc[n] = mfma(A1[m], B1[n - m], acc[n]);
            }
            LAS unsigned char* st = L + w * HC_CH + w * 32 + g * 136 + i * 8;
#pragma unroll
            for (int n = 0; n < 8; ++n)
                *(LAS v2u*)(st + n * 544) = (v2u){nosent(pk(acc[n][0], acc[n][1])), nosent(pk(acc[n][2], acc[n][3]))};
        }
        STAMP(6);
        lds_post(L, W_CB + w, 1);
        lds_wait4<false>(L, W_CB, 1);
        {
            const int cc = lane & 3;
#pragma unroll
            for (int it = 0; it < 4; ++it) {
                const int R = 64 * it + (t4 >> 2);
                const LAS unsigned char* sp = L + cc * HC_CH + cc * 32 + (R >> 5) * 544 + (R & 1) * 272 + ((R >> 1) & 15) * 8;
                const v2u a = *(const LAS v2u*)sp, c = *(const LAS v2u*)(sp + 136);
                __builtin_amdgcn_raw_buffer_store_b128((v4u){a.x, a.y, c.x, c.y}, ws, OFF_X2 + G * 16384 + ((R + 2 * G) & 255) * 64 + cc * 16, 0, SC1);
            }
        }
        STAMP(7);
        f32x2 cq[4][4], qn[4][4];
        float piv;
        {
            float m1[8], i1[8];
#pragma unroll
            for (int t = 0; t < 8; t += 2) { const f32x4 s = *(const LAS f32x4*)(L + L_ST1 + t * 8); m1[t] = s[0]; i1[t] = s[1]; m1[t + 1] = s[2]; i1[t + 1] = s[3]; }
            piv = *(const LAS float*)(L + L_ST1 + (lane & 7) * 8);
#pragma unroll
            for (int m = 0; m < 4; ++m) {
                const f32x2 mm = {m1[2 * m], m1[2 * m + 1]}, ii = {i1[2 * m], i1[2 * m + 1]};
                cq[0][m] = (f32x2){lo(qr[2 * m].x), lo(qr[2 * m + 1].x)} - mm;
                cq[1][m] = (f32x2){hi(qr[2 * m].x), hi(qr[2 * m + 1].x)} - mm;
                cq[2][m] = (f32x2){lo(qr[2 * m].y), lo(qr[2 * m + 1].y)} - mm;
                cq[3][m] = (f32x2){hi(qr[2 * m].y), hi(qr[2 * m + 1].y)} - mm;
#pragma unroll
                for (int c = 0; c < 4; ++c) qn[c][m] = cq[c][m] * ii;
            }
        }
        const float sumw = *(const LAS float*)(L + L_ST1 + 64);
        const float wc[4] = {lo(wrr.x), hi(wrr.x), lo(wrr.y), hi(wrr.y)};
        v4u y[4];
        {
            int off[4];
#pragma unroll
            for (int i = 0; i < 4; ++i) { const int Gs = 4 * (t4 >> 2) + i; off[i] = OFF_X2 + Gs * 16384 + ((G + 2 * Gs) & 255) * 64 + (t4 & 3) * 16; }
            poll16<4>(ws, off, y);
        }
        STAMP(8);
        f32x2 d[4][4];
        {
            f32x2 s1[4], s2[4], s3[4];
#pragma unroll
            for (int m = 0; m < 4; ++m) {
#pragma unroll
                for (int c = 0; c < 4; ++c) d[c][m] = __builtin_elementwise_fma(qn[c][m], (f32x2){lo(y[c][m]), hi(y[c][m])}, cq[c][m]);
                s1[m] = (d[0][m] + d[1][m]) + (d[2][m] + d[3][m]);
                s2[m] = d[0][m] * d[0][m];
                s3[m] = d[0][m] * wc[0];
#pragma unroll
                for (int c = 1; c < 4; ++c) { s2[m] = __builtin_elementwise_fma(d[c][m], d[c][m], s2[m]); s3[m] = __builtin_elementwise_fma(d[c][m], (f32x2){wc[c], wc[c]}, s3[m]); }
            }
            float z[3][2];
#pragma unroll
            for (int e = 0; e < 2; ++e) {
                z[0][e] = row_sum(fold16(fold32(s1[0][e], s1[2][e]), fold32(s1[1][e], s1[3][e])));
                z[1][e] = row_sum(fold16(fold32(s2[0][e], s2[2][e]), fold32(s2[1][e], s2[3][e])));
                z[2][e] = row_sum(fold16(fold32(s3[0][e], s3[2][e]), fold32(s3[1][e], s3[3][e])));
            }
            const int li = lane & 15;
            if (li < 6) {
                const int e = li & 1, zi = li >> 1;
                const float v0 = zi == 0 ? z[0][0] : (zi == 1 ? z[1][0] : z[2][0]);
                const float v1 = zi == 0 ? z[0][1] : (zi == 1 ? z[1][1] : z[2][1]);
                *(LAS float*)(L + L_RED + (((2 * (lane >> 4) + e) * 3 + zi) * 4 + w) * 4) = e ? v1 : v0;
            }
        }
        lds_post(L, W_CB + w, 2);
        lds_wait4<false>(L, W_CB, 2);
        float md, inv2, rr;
        {
            const LAS f32x4* rp = (const LAS f32x4*)(L + L_RED + (lane & 7) * 48);
            const f32x4 a = rp[0], c = rp[1], e = rp[2];
            const float S1 = (a[0] + a[1]) + (a[2] + a[3]), S2 = (c[0] + c[1]) + (c[2] + c[3]), S3 = (e[0] + e[1]) + (e[2] + e[3]);
            const float mean = S1 * (1.f / DM);
            const float var = fmaxf(S2 * (1.f / DM) - mean * mean, 0.f);
            md = rb(piv + mean) - piv;
            inv2 = __builtin_amdgcn_rcpf(rb(__builtin_amdgcn_sqrtf(rb(rb(var) + 1e-5f))));
            rr = (S3 - md * sumw) * inv2;
            rr = rr == rr ? rr : -INFINITY;
        }
        float mx = fmaxf(rr, dppf<0x124>(rr)); mx = fmaxf(mx, dppf<0x122>(mx)); mx = fmaxf(mx, dppf<0x121>(mx));
        const int tb = __builtin_ctzll(__ballot(rr == mx)) & 7;
        if (tid == 0) __builtin_amdgcn_raw_buffer_store_b64((v2u){__float_as_uint(mx), (unsigned)(8 * G + tb)}, ws, OFF_CAND + G * 8, 0, SC1);
        v2u ocand;
        {
            float ds[4];
#pragma unroll
            for (int c = 0; c < 4; ++c) {
                const f32x8 dd = {d[c][0].x, d[c][0].y, d[c][1].x, d[c][1].y, d[c][2].x, d[c][2].y, d[c][3].x, d[c][3].y};
                ds[c] = dd[tb];
            }
            const float mdb = rdl(md, tb), ivb = rdl(inv2, tb), pvb = rdl(piv, tb);
            const unsigned n0 = nosent(pk((ds[0] - mdb) * ivb, (ds[1] - mdb) * ivb)), n1 = nosent(pk((ds[2] - mdb) * ivb, (ds[3] - mdb) * ivb));
            const unsigned h0 = nosent(pk(ds[0] + pvb, ds[1] + pvb)), h1 = nosent(pk(ds[2] + pvb, ds[3] + pvb));
            const bool odd = lane & 1;
            const unsigned e0 = dppu<0xB1>(odd ? n0 : h0), e1 = dppu<0xB1>(odd ? n1 : h1);
            const v4u o = odd ? (v4u){e0, e1, h0, h1} : (v4u){n0, n1, e0, e1};
            __builtin_amdgcn_raw_buffer_store_b128(o, ws, OFF_ROWS + G * 4096 + (odd ? 2048 : 0) + (t4 >> 1) * 16, 0, SC1);
            ocand = (v2u){pk(lo(h0) + lo(n0), hi(h0) + hi(n0)), pk(lo(h1) + lo(n1), hi(h1) + hi(n1))};
        }
        STAMP(9);
        const rsrc_t ro = mk(p.out, SEQ * DM * 2);
        {
#pragma unroll
            for (int m = 0; m < 4; ++m) {
                const float i0 = rdl(inv2, 2 * m), i1 = rdl(inv2, 2 * m + 1);
                const f32x2 sc = {1.f + i0, 1.f + i1};
                const f32x2 of = {rdl(piv, 2 * m) - rdl(md, 2 * m) * i0, rdl(piv, 2 * m + 1) - rdl(md, 2 * m + 1) * i1};
                f32x2 o[4];
#pragma unroll
                for (int c = 0; c < 4; ++c) o[c] = __builtin_elementwise_fma(d[c][m], sc, of);
                const unsigned a0 = pk(o[0].x, o[1].x), a1 = pk(o[2].x, o[3].x), c0 = pk(o[0].y, o[1].y), c1 = pk(o[2].y, o[3].y);
                const bool odd = lane & 1;
                const unsigned e0 = dppu<0xB1>(odd ? a0 : c0), e1 = dppu<0xB1>(odd ? a1 : c1);
                const v4u ov = odd ? (v4u){e0, e1, c0, c1} : (v4u){a0, a1, e0, e1};
                const int t = 2 * m + (int)odd;
                if (t != tb) __builtin_amdgcn_raw_buffer_store_b128(ov, ro, (8 * G + t) * 2048 + (t4 >> 1) * 16, 0, SC1);
            }
        }
        const unsigned win1 = lds_wait1<true>(L, W_WIN);
        const int win = (int)(win1 - 1) & 2047;
        if (win != 8 * G + tb) __builtin_amdgcn_raw_buffer_store_b64(ocand, ro, (8 * G + tb) * 2048 + t4 * 8, 0, SC1);
        STAMP(10);
        if (wave == 0) {
            const float rw = __uint_as_float(*(volatile LAS unsigned*)(L + L_SYNC + 4 * (W_WIN + 1)));
            const int col = 2 * (lane >> 5) + ((lane >> 4) & 1);
            unsigned hn = SENT, hh = SENT;
#pragma nounroll
            for (unsigned s = 0; s < SPIN_G; ++s) {
                hn = __builtin_amdgcn_raw_buffer_load_b32(ws, OFF_ROWS + (win >> 3) * 4096 + G * 8 + (col >> 1) * 4, 0, SC1);
                hh = __builtin_amdgcn_raw_buffer_load_b32(ws, OFF_ROWS + (win >> 3) * 4096 + 2048 + G * 8 + (col >> 1) * 4, 0, SC1);
                if (hn != SENT && hh != SENT) break;
                CFENCE();
            }
            int off[4]; v4u x[4];
#pragma unroll
            for (int i = 0; i < 4; ++i) { const int f = lane + 64 * i; off[i] = f < NFS ? OFF_PART + f * 4096 + G * 16 : (int)0x80000000; }
            poll16<4>(ws, off, x);
            float s[4];
#pragma unroll
            for (int c = 0; c < 4; ++c) s[c] = (__uint_as_float(x[0][c]) + __uint_as_float(x[1][c])) + (__uint_as_float(x[2][c]) + __uint_as_float(x[3][c]));
            const float tot = row_sum(fold16(fold32(s[0], s[2]), fold32(s[1], s[3])));
            const float sg = rb(__builtin_amdgcn_rcpf(1.f + __expf(-rb(rw))));
            const float yv = rb(rb(tot) * sg);
            const float n = (col & 1) ? hi(hn) : lo(hn), h = (col & 1) ? hi(hh) : lo(hh);
            if ((lane & 15) == 0) __builtin_amdgcn_raw_buffer_store_b16((u16)pk(h + rb(n + yv), 0.f), ro, win * 2048 + G * 8 + col * 2, 0, SC1);
        }
        STAMP(11);
    } else {
        if (wave == 4) *(LAS unsigned*)(L + L_SYNC + lane * 4) = 0u;
        const int fs = (G & 31) < 28 ? (G >> 5) * 28 + (G & 31) : -1;
        const rsrc_t rh = mk(p.hk + chc * SEQ, SEQ * 2);
        v4u ha[5], hb[5];
#pragma unroll
        for (int i = 0; i < 5; ++i) {
            const int c = lane + 64 * i;
            ha[i] = __builtin_amdgcn_raw_buffer_load_b128(rh, c >= 2 ? c * 16 - 32 : (int)0x80000000, 0, 0);
            hb[i] = __builtin_amdgcn_raw_buffer_load_b128(rh, c >= 1 ? c * 16 - 16 : (int)0x80000000, 0, 0);
        }
        const v2u ep = __builtin_amdgcn_raw_buffer_load_b64(ws, OFF_EPOCH, 0, SC1);
        const v4u ss = {SENT, SENT, SENT, SENT};
#pragma unroll
        for (int c = 0; c < 4; ++c)
            __builtin_amdgcn_raw_buffer_store_b128(ss, ws, OFF_X1 + (ch0 + c) * 4096 + ((tg + ch0 + c) & 31) * 128 + rg * 16, 0, SC1);
#pragma unroll
        for (int c = 0; c < 4; ++c) __builtin_amdgcn_raw_buffer_store_b128(ss, ws, OFF_X2 + G * 16384 + c * 4096 + t4 * 16, 0, SC1);
        __builtin_amdgcn_raw_buffer_store_b128(ss, ws, OFF_ROWS + G * 4096 + t4 * 16, 0, SC1);
        if (fs >= 0) __builtin_amdgcn_raw_buffer_store_b128(ss, ws, OFF_PART + fs * 4096 + t4 * 16, 0, SC1);
        if (t4 == 0) __builtin_amdgcn_raw_buffer_store_b64((v2u){SENT, SENT}, ws, OFF_CAND + G * 8, 0, SC1);
        const GAS v4u* qp = (const GAS v4u*)(p.q + (8 * G + 2 * w) * DM);
        const v4u q0 = qp[lane], q1 = qp[64 + lane], q2 = qp[128 + lane], q3 = qp[192 + lane];
        const v4u w0 = ((const GAS v4u*)p.wr)[lane], w1 = ((const GAS v4u*)p.wr)[64 + lane];
        asm volatile("s_waitcnt lgkmcnt(0)" ::: "memory");
        __builtin_amdgcn_s_barrier();
        asm volatile("s_waitcnt vmcnt(0)" ::: "memory");
        STAMP(1);
        const u64 tag = (((u64)ep.y << 32) | ep.x) + 1;
        const v2u tagv = {(unsigned)tag, (unsigned)(tag >> 32)};
        if (lane == 0) __builtin_amdgcn_raw_buffer_store_b64(tagv, ws, OFF_FL + (G * 4 + w) * 8, 0, SC1);
        lds_post(L, W_CLR + w, 1);
        {
            LAS unsigned char* hp = L + w * HC_CH + lane * 16;
#pragma unroll
            for (int i = 0; i < 5; ++i) {
                const unsigned e[8] = {ha[i].x, ha[i].y, ha[i].z, ha[i].w, hb[i].x, hb[i].y, hb[i].z, hb[i].w};
                unsigned o[7];
#pragma unroll
                for (int j = 0; j < 7; ++j) o[j] = __builtin_amdgcn_alignbit(e[j + 1], e[j], 16);
                if (i < 4 || lane < 4) {
#pragma unroll
                    for (int a = 0; a < 4; ++a) {
                        *(LAS v4u*)(hp + (2 * a) * HC_COPY + i * 1024) = (v4u){o[a], o[a + 1], o[a + 2], o[a + 3]};
                        *(LAS v4u*)(hp + (2 * a + 1) * HC_COPY + i * 1024) = (v4u){e[a + 1], e[a + 2], e[a + 3], e[a + 4]};
                    }
                }
            }
        }
        {
            float mv[2], iv[2];
#pragma unroll
            for (int r = 0; r < 2; ++r) {
                const v4u a = r ? q2 : q0, c = r ? q3 : q1;
                float x[16];
#pragma unroll
                for (int j = 0; j < 4; ++j) { x[2 * j] = lo(a[j]); x[2 * j + 1] = hi(a[j]); x[8 + 2 * j] = lo(c[j]); x[9 + 2 * j] = hi(c[j]); }
                float s = 0.f;
#pragma unroll
                for (int j = 0; j < 16; ++j) s += x[j];
                const float mean = wave_sum(s) * (1.f / DM);
                float vv = 0.f;
#pragma unroll
                for (int j = 0; j < 16; ++j) vv += (x[j] - mean) * (x[j] - mean);
                const float var = wave_sum(vv) * (1.f / DM);
                mv[r] = rb(mean);
                iv[r] = __builtin_amdgcn_rcpf(rb(__builtin_amdgcn_sqrtf(rb(rb(var) + 1e-5f))));
            }
            float s = 0.f;
#pragma unroll
            for (int j = 0; j < 4; ++j) s += (lo(w0[j]) + hi(w0[j])) + (lo(w1[j]) + hi(w1[j]));
            s = wave_sum(s);
            if (lane == 0) {
                *(LAS f32x4*)(L + L_ST1 + w * 16) = (f32x4){mv[0], iv[0], mv[1], iv[1]};
                if (w == 0) *(LAS float*)(L + L_ST1 + 64) = s;
            }
        }
        lds_post(L, W_HC + w, 1);
        STAMP(2);
#pragma nounroll
        for (unsigned s = 0; s < SPIN_G; ++s) {
            const v4u f0 = __builtin_amdgcn_raw_buffer_load_b128(ws, OFF_FL + w * 2048 + lane * 16, 0, SC1);
            const v4u f1 = __builtin_amdgcn_raw_buffer_load_b128(ws, OFF_FL + w * 2048 + 1024 + lane * 16, 0, SC1);
            const bool ok = f0.x == tagv.x && f0.y == tagv.y && f0.z == tagv.x && f0.w == tagv.y && f1.x == tagv.x && f1.y == tagv.y && f1.z == tagv.x && f1.w == tagv.y;
            if (__all(ok)) break;
            CFENCE();
        }
        lds_post(L, W_OK + w, 1);
        STAMP(3);
        v4u bg[8], bu[8];
        unsigned wdp[8][4];
        if (fs >= 0) {
            const rsrc_t rg_ = mk(p.wg, DM * FF * 2), ru_ = mk(p.wu, DM * FF * 2), rd_ = mk(p.wd, DM * FF * 2);
            const int go = (256 * w + (lane >> 1)) * (FF * 2) + fs * 32 + (lane & 1) * 16;
            v4u g_[8], u_[8]; v2u dn[16];
#pragma unroll
            for (int i = 0; i < 8; ++i) {
                g_[i] = __builtin_amdgcn_raw_buffer_load_b128(rg_, go, i * 32 * FF * 2, 0);
                u_[i] = __builtin_amdgcn_raw_buffer_load_b128(ru_, go, i * 32 * FF * 2, 0);
            }
#pragma unroll
            for (int i = 0; i < 16; ++i) dn[i] = __builtin_amdgcn_raw_buffer_load_b64(rd_, fs * 16 * 2048 + t4 * 8, i * 2048, 0);
            LAS unsigned char* tp = L + L_TR + w * 1024;
            const LAS unsigned char* tr = tp + (8 * (lane >> 4) + ((lane & 15) >> 2)) * 32 + (lane & 3) * 8;
#pragma unroll
            for (int i = 0; i < 16; ++i) {
                *(LAS v4u*)(tp + lane * 16) = (i & 1) ? u_[i >> 1] : g_[i >> 1];
                CFENCE();
                const v2u a = __builtin_bit_cast(v2u, __builtin_amdgcn_ds_read_tr16_b64_v4i16((LAS s16x4*)tr));
                const v2u c = __builtin_bit_cast(v2u, __builtin_amdgcn_ds_read_tr16_b64_v4i16((LAS s16x4*)(tr + 128)));
                CFENCE();
                if (i & 1) bu[i >> 1] = (v4u){a.x, a.y, c.x, c.y}; else bg[i >> 1] = (v4u){a.x, a.y, c.x, c.y};
            }
#pragma unroll
            for (int m = 0; m < 8; ++m) {
                wdp[m][0] = __builtin_amdgcn_perm(dn[2 * m + 1].x, dn[2 * m].x, 0x05040100);
                wdp[m][1] = __builtin_amdgcn_perm(dn[2 * m + 1].x, dn[2 * m].x, 0x07060302);
                wdp[m][2] = __builtin_amdgcn_perm(dn[2 * m + 1].y, dn[2 * m].y, 0x05040100);
                wdp[m][3] = __builtin_amdgcn_perm(dn[2 * m + 1].y, dn[2 * m].y, 0x07060302);
            }
        }
        STAMP(4);
        lds_wait4<true>(L, W_OK, 1);
        if (G == 0 && tid == 256) __builtin_amdgcn_raw_buffer_store_b64(tagv, ws, OFF_EPOCH, 0, SC1);
        if (fs >= 0 || w == 0) {
            lds_wait4<true>(L, W_CB, 2);
            __builtin_amdgcn_s_setprio(3);
            STAMP(5);
            int off[2] = {OFF_CAND + lane * 32, OFF_CAND + lane * 32 + 16};
            v4u c[2];
            poll16<2>(ws, off, c);
            float br = __uint_as_float(c[0].x); unsigned bi = c[0].y;
            if (__uint_as_float(c[0].z) > br) { br = __uint_as_float(c[0].z); bi = c[0].w; }
            if (__uint_as_float(c[1].x) > br) { br = __uint_as_float(c[1].x); bi = c[1].y; }
            if (__uint_as_float(c[1].z) > br) { br = __uint_as_float(c[1].z); bi = c[1].w; }
            const float mx = wave_max(br);
            const int win = __builtin_amdgcn_readlane(bi, __builtin_ctzll(__ballot(br == mx)) & 63) & 2047;
            STAMP(6);
            if (w == 0) { lds_post(L, W_WIN + 1, __float_as_uint(mx)); lds_post(L, W_WIN, win + 1); }
            if (fs >= 0) {
                int xo[8]; v4u xa[8];
#pragma unroll
                for (int i = 0; i < 8; ++i) xo[i] = OFF_ROWS + (win >> 3) * 4096 + (256 * w + 32 * i + 8 * (lane >> 4)) * 2;
                poll16<8>(ws, xo, xa);
                STAMP(7);
                f32x4 ag = {0.f, 0.f, 0.f, 0.f}, au = {0.f, 0.f, 0.f, 0.f};
#pragma unroll
                for (int i = 0; i < 8; ++i) { ag = mfma(xa[i], bg[i], ag); au = mfma(xa[i], bu[i], au); }
                if (lane < 32) *(LAS float*)(L + L_GU + lane * 16 + w * 4) = lane < 16 ? ag[0] : au[0];
                lds_post(L, W_HB + w, 1);
                lds_wait4<false>(L, W_HB, 1);
                const f32x4 pg = *(const LAS f32x4*)(L + L_GU + (lane & 15) * 16), pu = *(const LAS f32x4*)(L + L_GU + 256 + (lane & 15) * 16);
                const float g = rb((pg[0] + pg[1]) + (pg[2] + pg[3])), u = rb((pu[0] + pu[1]) + (pu[2] + pu[3]));
                const float a = rb(g * __builtin_amdgcn_rcpf(1.f + __expf(-g))) * u;
                const unsigned ap = pk(a, dppf<0xB1>(a));
                float o[4] = {0.f, 0.f, 0.f, 0.f};
#pragma unroll
                for (int m = 0; m < 8; ++m) {
                    const unsigned am = __builtin_amdgcn_readlane(ap, 2 * m);
#pragma unroll
                    for (int c2 = 0; c2 < 4; ++c2)
                        o[c2] = __builtin_amdgcn_fdot2_f32_bf16(__builtin_bit_cast(bf16x2, wdp[m][c2]), __builtin_bit_cast(bf16x2, am), o[c2], false);
                }
                __builtin_amdgcn_raw_buffer_store_b128((v4u){nosent(__float_as_uint(o[0])), nosent(__float_as_uint(o[1])), nosent(__float_as_uint(o[2])), nosent(__float_as_uint(o[3]))},
                                                       ws, OFF_PART + fs * 4096 + t4 * 16, 0, SC1);
                STAMP(8);
            }
        }
    }
}

extern "C" void kernel_launch(void* const* d_in, const int* in_sizes, int n_in, void* d_out, int out_size, void* d_ws, size_t ws_size,
                              hipStream_t stream) {
    Params p;
    p.q = (const u16*)d_in[0]; p.k = (const u16*)d_in[1]; p.v = (const u16*)d_in[2]; p.sk = (const u16*)d_in[3];
    p.hk = (const u16*)d_in[4]; p.dv = (const u16*)d_in[5]; p.wr = (const u16*)d_in[7]; p.wg = (const u16*)d_in[8];
    p.wu = (const u16*)d_in[9]; p.wd = (const u16*)d_in[10];
    p.out = (u16*)d_out; p.ws = (unsigned char*)d_ws;
    (void)hipFuncSetAttribute((const void*)h3_mod_kernel, hipFuncAttributeMaxDynamicSharedMemorySize, LDS_BYTES);
    h3_mod_kernel<<<NWG, NTHR, LDS_BYTES, stream>>>(p);
}
```

```cpp
#include <hip/hip_runtime.h>
#include <hip/hip_bf16.h>

#define GAS __attribute__((address_space(1)))
#define LAS __attribute__((address_space(3)))
typedef unsigned short u16;
typedef unsigned long long u64;
typedef unsigned v4u __attribute__((ext_vector_type(4)));
typedef unsigned v2u __attribute__((ext_vector_type(2)));
typedef float f32x4 __attribute__((ext_vector_type(4)));
typedef float f32x2 __attribute__((ext_vector_type(2)));
typedef float f32x8 __attribute__((ext_vector_type(8)));
typedef __bf16 bf16x2 __attribute__((ext_vector_type(2)));
typedef __bf16 bf16x8 __attribute__((ext_vector_type(8)));
typedef short s16x4 __attribute__((ext_vector_type(4)));
typedef __amdgpu_buffer_rsrc_t rsrc_t;

constexpr int SEQ = 2048, DM = 1024, FF = 3584;
constexpr int NWG = 256, NTHR = 512, NFS = 224;
constexpr unsigned SENT = 0xFFFFFFFFu;
constexpr int SC1 = 16;

constexpr int OFF_EPOCH = 0;
constexpr int OFF_FL = 16384;
constexpr int OFF_CAND = 32768;
constexpr int OFF_PART = 65536;
constexpr int OFF_ROWS = 1 << 20;
constexpr int OFF_X1 = 2 << 20;
constexpr int OFF_X2 = 6 << 20;
constexpr int NT = 2;
#define WS_BASE 0
#define CODE_BYTES 17920
#define WGU_AUX 0
#define OUT_AUX SC1
constexpr int OFF_DBG = 16 << 20;
constexpr int WS_USED = 32 << 20;

constexpr int HC_COPY = 260 * 16;
constexpr int HC_CH = 8 * HC_COPY;
constexpr int XB_OFF = 4 * HC_CH;
constexpr int XB_CH = 290 * 16;
constexpr int MISC = XB_OFF + 4 * XB_CH;
constexpr int L_SYNC = MISC;
constexpr int L_ST1 = MISC + 256;
constexpr int L_RED = MISC + 512;
constexpr int L_GU = MISC + 1024;
constexpr int L_TR = MISC + 2048;
constexpr int LDS_BYTES = L_TR + 4096;
constexpr int W_CLR = 0, W_OK = 4, W_HC = 8, W_CB = 12, W_HB = 16, W_WIN = 20, W_WR = 24;

constexpr unsigned SPIN_G = 1u << 18, SPIN_L = 1u << 22;

#define STAMP(i) do {} while (0)

struct Params {
    const u16 *q, *k, *v, *sk, *hk, *dv, *wr, *wg, *wu, *wd;
    u16* out;
    unsigned char* ws;
};

__device__ __forceinline__ rsrc_t mk(const void* p, unsigned bytes) { return __builtin_amdgcn_make_buffer_rsrc((void*)p, 0, bytes, 0x00020000); }
__device__ __forceinline__ float lo(unsigned x) { return __uint_as_float(x << 16); }
__device__ __forceinline__ float hi(unsigned x) { return __uint_as_float(x & 0xFFFF0000u); }
__device__ __forceinline__ unsigned pk(float a, float b) { f32x2 t = {a, b}; return __builtin_bit_cast(unsigned, __builtin_convertvector(t, bf16x2)); }
__device__ __forceinline__ float rb(float a) { return lo(pk(a, a) & 0xFFFFu); }
__device__ __forceinline__ unsigned nosent(unsigned x) { return x < 0xFFFFFFFEu ? x : 0xFFFFFFFEu; }
__device__ __forceinline__ unsigned umax(unsigned a, unsigned b) { return a > b ? a : b; }
__device__ __forceinline__ bool valid4(v4u x) { return umax(umax(x.x, x.y), umax(x.z, x.w)) != SENT; }
__device__ __forceinline__ f32x4 mfma(v4u a, v4u b, f32x4 c) {
    return __builtin_amdgcn_mfma_f32_16x16x32_bf16(__builtin_bit_cast(bf16x8, a), __builtin_bit_cast(bf16x8, b), c, 0, 0, 0);
}
template <int CTRL> __device__ __forceinline__ float dppf(float x) {
    return __uint_as_float(__builtin_amdgcn_update_dpp(0u, __float_as_uint(x), CTRL, 0xF, 0xF, true));
}
template <int CTRL> __device__ __forceinline__ unsigned dppu(unsigned x) { return __builtin_amdgcn_update_dpp(0u, x, CTRL, 0xF, 0xF, true); }
__device__ __forceinline__ float row_sum(float x) {
    x += dppf<0x128>(x); x += dppf<0x124>(x); x += dppf<0x122>(x); x += dppf<0x121>(x);
    return x;
}
__device__ __forceinline__ float fold32(float a, float b) {
    auto r = __builtin_amdgcn_permlane32_swap(__float_as_uint(a), __float_as_uint(b), false, false);
    return __uint_as_float(r[0]) + __uint_as_float(r[1]);
}
__device__ __forceinline__ float fold16(float a, float b) {
    auto r = __builtin_amdgcn_permlane16_swap(__float_as_uint(a), __float_as_uint(b), false, false);
    return __uint_as_float(r[0]) + __uint_as_float(r[1]);
}
__device__ __forceinline__ float wave_sum(float x) { x = fold32(x, x); x = fold16(x, x); return row_sum(x); }
__device__ __forceinline__ float wave_max(float x) {
    auto r = __builtin_amdgcn_permlane32_swap(__float_as_uint(x), __float_as_uint(x), false, false);
    x = fmaxf(__uint_as_float(r[0]), __uint_as_float(r[1]));
    auto s = __builtin_amdgcn_permlane16_swap(__float_as_uint(x), __float_as_uint(x), false, false);
    x = fmaxf(__uint_as_float(s[0]), __uint_as_float(s[1]));
    x = fmaxf(x, dppf<0x128>(x)); x = fmaxf(x, dppf<0x124>(x)); x = fmaxf(x, dppf<0x122>(x)); x = fmaxf(x, dppf<0x121>(x));
    return x;
}
__device__ __forceinline__ float rdl(float x, int l) { return __uint_as_float(__builtin_amdgcn_readlane(__float_as_uint(x), l)); }

#define CFENCE() asm volatile("" ::: "memory")
__device__ __forceinline__ void lds_post(LAS unsigned char* L, int word, unsigned val) {
    CFENCE();
    *(volatile LAS unsigned*)(L + L_SYNC + 4 * word) = val;
    CFENCE();
}
template <bool SLEEP> __device__ __forceinline__ void lds_wait4(LAS unsigned char* L, int group, unsigned val) {
#pragma nounroll
    for (unsigned s = 0; s < SPIN_L; ++s) {
        const v4u x = *(volatile LAS v4u*)(L + L_SYNC + 4 * group);
        const unsigned m = __builtin_amdgcn_readfirstlane(min(min(x.x, x.y), min(x.z, x.w)));
        if (m >= val) break;
        if (SLEEP) __builtin_amdgcn_s_sleep(1);
    }
    CFENCE();
}
template <bool SLEEP> __device__ __forceinline__ unsigned lds_wait1(LAS unsigned char* L, int word) {
    unsigned m = 0;
#pragma nounroll
    for (unsigned s = 0; s < SPIN_L; ++s) {
        m = __builtin_amdgcn_readfirstlane(*(volatile LAS unsigned*)(L + L_SYNC + 4 * word));
        if (m != 0) break;
        if (SLEEP) __builtin_amdgcn_s_sleep(1);
    }
    CFENCE();
    return m;
}
template <int N> __device__ __forceinline__ void poll16(rsrc_t rs, const int (&off)[N], v4u (&x)[N]) {
#pragma nounroll
    for (unsigned s = 0; s < SPIN_G; ++s) {
        bool ok = true;
#pragma unroll
        for (int i = 0; i < N; ++i) x[i] = __builtin_amdgcn_raw_buffer_load_b128(rs, off[i], 0, SC1);
#pragma unroll
        for (int i = 0; i < N; ++i) ok &= valid4(x[i]);
        if (ok) break;
        CFENCE();
    }
    CFENCE();
}

__global__ void __launch_bounds__(NTHR, 2) h3_mod_kernel(Params p) {
    extern __shared__ __attribute__((aligned(16))) unsigned char smem[];
    LAS unsigned char* L = (LAS unsigned char*)smem;
    u64 pc0;
    asm volatile("s_getpc_b64 %0" : "=s"(pc0));
    const int tid = threadIdx.x, lane = tid & 63;
    const int wave = __builtin_amdgcn_readfirstlane(tid >> 6), w = wave & 3;
    const int b = blockIdx.x, G = ((b & 7) << 5) | (b >> 3);
    const int t4 = 64 * w + lane;
    STAMP(0);

    if (__builtin_expect(wave < 4, 1)) {
        const rsrc_t rk = mk(p.k, SEQ * DM * 2), rv = mk(p.v, SEQ * DM * 2);
        const int vo = G * 16384 + t4 * 8;
        const int vh = G ? vo - 3 * 2048 : (int)0x80000000;
        v2u kr[11], vr[8], qr[8];
#pragma unroll
        for (int i = 0; i < 8; ++i) {
            kr[3 + i] = __builtin_amdgcn_raw_buffer_load_b64(rk, vo, i * 2048, NT);
            vr[i] = __builtin_amdgcn_raw_buffer_load_b64(rv, vo, i * 2048, NT);
        }
#pragma unroll
        for (int i = 0; i < 3; ++i) kr[i] = __builtin_amdgcn_raw_buffer_load_b64(rk, vh, i * 2048, NT);
        const v4u sa = *(const GAS v4u*)(p.sk + t4 * 16), sb = *(const GAS v4u*)(p.sk + t4 * 16 + 8);
        __builtin_amdgcn_sched_barrier(0);
        __builtin_amdgcn_s_setprio(2);
        const rsrc_t ws = mk(p.ws, WS_USED);
        if (lane < 30) *(LAS v4u*)(L + XB_OFF + w * XB_CH + lane * 16) = (v4u){0u, 0u, 0u, 0u};
        asm volatile("s_waitcnt lgkmcnt(0)" ::: "memory");
        __builtin_amdgcn_s_barrier();
        CFENCE();
        v4u kv[4];
        {
            const f32x2 w01[4] = {{lo(sa.x), lo(sa.z)}, {hi(sa.x), hi(sa.z)}, {lo(sa.y), lo(sa.w)}, {hi(sa.y), hi(sa.w)}};
            const f32x2 w23[4] = {{lo(sb.x), lo(sb.z)}, {hi(sb.x), hi(sb.z)}, {lo(sb.y), lo(sb.w)}, {hi(sb.y), hi(sb.w)}};
            f32x2 ka[11], kb[11], xa[8], xb[8];
#pragma unroll
            for (int i = 0; i < 11; ++i) { ka[i] = (f32x2){lo(kr[i].x), hi(kr[i].x)}; kb[i] = (f32x2){lo(kr[i].y), hi(kr[i].y)}; }
#pragma unroll
            for (int t = 0; t < 8; ++t) {
                f32x2 a = ka[t] * w01[3], c = kb[t] * w23[3];
#pragma unroll
                for (int j = 2; j >= 0; --j) { a = __builtin_elementwise_fma(ka[t + 3 - j], w01[j], a); c = __builtin_elementwise_fma(kb[t + 3 - j], w23[j], c); }
                xa[t] = a * (f32x2){lo(vr[t].x), hi(vr[t].x)};
                xb[t] = c * (f32x2){lo(vr[t].y), hi(vr[t].y)};
            }
#pragma unroll
            for (int m = 0; m < 4; ++m) {
                kv[0][m] = nosent(pk(xa[7 - 2 * m].x, xa[6 - 2 * m].x));
                kv[1][m] = nosent(pk(xa[7 - 2 * m].y, xa[6 - 2 * m].y));
                kv[2][m] = nosent(pk(xb[7 - 2 * m].x, xb[6 - 2 * m].x));
                kv[3][m] = nosent(pk(xb[7 - 2 * m].y, xb[6 - 2 * m].y));
            }
            STAMP(1);
            lds_wait4<false>(L, W_CLR, 1);
#pragma unroll
            for (int c = 0; c < 4; ++c)
                __builtin_amdgcn_raw_buffer_store_b128(kv[c], ws, OFF_X1 + G * 16384 + ((w * 4096 + c * 1024 + lane * 16 + G * 128) & 16383), 0, SC1);
        }
        STAMP(2);
        lds_wait4<false>(L, W_OK, 1);
        STAMP(3);
        {
            int off[4]; v4u x[4];
            const int seg = (G >> 6) * 4096 + (G & 3) * 1024 + ((G >> 2) & 15) * 64 + (lane & 3) * 16;
#pragma unroll
            for (int i = 0; i < 4; ++i) { const int R = 64 * i + (t4 >> 2); off[i] = OFF_X1 + R * 16384 + ((seg + R * 128) & 16383); }
            poll16<4>(ws, off, x);
#pragma unroll
            for (int i = 0; i < 4; ++i) *(LAS v4u*)(L + XB_OFF + (lane & 3) * XB_CH + (30 + 64 * i + (t4 >> 2)) * 16) = x[i];
        }
        lds_post(L, W_CB + w, 1);
        STAMP(4);
        {
            const rsrc_t rq = mk(p.q, SEQ * DM * 2);
#pragma unroll
            for (int i = 0; i < 8; ++i) qr[i] = __builtin_amdgcn_raw_buffer_load_b64(rq, vo, i * 2048, 0);
        }
        const v2u wrr = *(const GAS v2u*)(p.wr + t4 * 4), dvr = *(const GAS v2u*)(p.dv + t4 * 4);
        lds_wait1<false>(L, W_HC + w);
        lds_wait4<false>(L, W_CB, 1);
        STAMP(5);
        {
            f32x4 acc[8];
            const int i = lane & 15, g = lane >> 4;
            const LAS unsigned char* ap = L + w * HC_CH + (i & 7) * HC_COPY + (3 - g + (i >> 3)) * 16;
            const LAS unsigned char* bp = L + XB_OFF + w * XB_CH + (28 + 2 * i + g) * 16;
            v4u A0[8], B0[8], A1[8], B1[8];
#pragma unroll
            for (int u = 0; u < 8; ++u) { A0[u] = *(const LAS v4u*)(ap + 512 * u); B0[u] = *(const LAS v4u*)(bp + 512 * u); }
#pragma unroll
            for (int n = 0; n < 8; ++n) acc[n] = (f32x4){0.f, 0.f, 0.f, 0.f};
#pragma unroll 1
            for (int r2 = 0; r2 < 4; ++r2) {
#pragma unroll
                for (int u = 0; u < 8; ++u) { B1[u] = *(const LAS v4u*)(bp - 64 + 512 * u); A1[u] = *(const LAS v4u*)(ap + 64 + 512 * u); }
#pragma unroll
                for (int m = 0; m < 8; ++m)
#pragma unroll
                    for (int n = m; n < 8; ++n) acc[n] = mfma(A0[m], B0[n - m], acc[n]);
                ap += 128; bp -= 128;
                if (r2 < 3) {
#pragma unroll
                    for (int u = 0; u < 8; ++u) { B0[u] = *(const LAS v4u*)(bp + 512 * u); A0[u] = *(const LAS v4u*)(ap + 512 * u); }
                }
#pragma unroll
                for (int m = 0; m < 8; ++m)
#pragma unroll
                    for (int n = m; n < 8; ++n) acc[n] = mfma(A1[m], B1[n - m], acc[n]);
            }
            LAS unsigned char* st = L + w * HC_CH + w * 32 + g * 136 + i * 8;
#pragma unroll
            for (int n = 0; n < 8; ++n)
                *(LAS v2u*)(st + n * 544) = (v2u){nosent(pk(acc[n][0], acc[n][1])), nosent(pk(acc[n][2], acc[n][3]))};
        }
        STAMP(6);
        lds_post(L, W_CB + w, 2);
        lds_wait4<false>(L, W_CB, 2);
        {
            const int cc = lane & 3;
#pragma unroll
            for (int it = 0; it < 4; ++it) {
                const int R = 64 * it + (t4 >> 2);
                const LAS unsigned char* sp = L + cc * HC_CH + cc * 32 + (R >> 5) * 544 + (R & 1) * 272 + ((R >> 1) & 15) * 8;
                const v2u a = *(const LAS v2u*)sp, c = *(const LAS v2u*)(sp + 136);
                __builtin_amdgcn_raw_buffer_store_b128((v4u){a.x, a.y, c.x, c.y}, ws, OFF_X2 + G * 16384 + ((R + 2 * G) & 255) * 64 + cc * 16, 0, SC1);
            }
        }
        STAMP(7);
        f32x2 bs[4][4], qn[4][4];
        float piv;
        {
            float m1[8], i1[8];
#pragma unroll
            for (int t = 0; t < 8; t += 2) { const f32x4 s = *(const LAS f32x4*)(L + L_ST1 + t * 8); m1[t] = s[0]; i1[t] = s[1]; m1[t + 1] = s[2]; i1[t + 1] = s[3]; }
            piv = *(const LAS float*)(L + L_ST1 + (lane & 7) * 8);
            const float dc[4] = {lo(dvr.x), hi(dvr.x), lo(dvr.y), hi(dvr.y)};
#pragma unroll
            for (int m = 0; m < 4; ++m) {
                const f32x2 mm = {m1[2 * m], m1[2 * m + 1]}, ii = {i1[2 * m], i1[2 * m + 1]};
                f32x2 cq[4];
                cq[0] = (f32x2){lo(qr[2 * m].x), lo(qr[2 * m + 1].x)} - mm;
                cq[1] = (f32x2){hi(qr[2 * m].x), hi(qr[2 * m + 1].x)} - mm;
                cq[2] = (f32x2){lo(qr[2 * m].y), lo(qr[2 * m + 1].y)} - mm;
                cq[3] = (f32x2){hi(qr[2 * m].y), hi(qr[2 * m + 1].y)} - mm;
#pragma unroll
                for (int c = 0; c < 4; ++c) {
                    qn[c][m] = cq[c] * ii;
                    const f32x2 kd = (f32x2){hi(kv[c][3 - m]), lo(kv[c][3 - m])} * dc[c];
                    bs[c][m] = __builtin_elementwise_fma(qn[c][m], kd, cq[c]);
                }
            }
        }
        const float sumw = *(const LAS float*)(L + L_ST1 + 64);
        const float wc[4] = {lo(wrr.x), hi(wrr.x), lo(wrr.y), hi(wrr.y)};
        __builtin_amdgcn_sched_barrier(0);
        v4u y[4];
        {
            int off[4];
#pragma unroll
            for (int i = 0; i < 4; ++i) { const int Gs = 4 * (t4 >> 2) + i; off[i] = OFF_X2 + Gs * 16384 + ((G + 2 * Gs) & 255) * 64 + (t4 & 3) * 16; }
            poll16<4>(ws, off, y);
        }
        STAMP(8);
        f32x2 d[4][4];
        {
            f32x2 s1[4], s2[4], s3[4];
#pragma unroll
            for (int m = 0; m < 4; ++m) {
#pragma unroll
                for (int c = 0; c < 4; ++c) d[c][m] = __builtin_elementwise_fma(qn[c][m], (f32x2){lo(y[c][m]), hi(y[c][m])}, bs[c][m]);
                s1[m] = (d[0][m] + d[1][m]) + (d[2][m] + d[3][m]);
                s2[m] = d[0][m] * d[0][m];
                s3[m] = d[0][m] * wc[0];
#pragma unroll
                for (int c = 1; c < 4; ++c) { s2[m] = __builtin_elementwise_fma(d[c][m], d[c][m], s2[m]); s3[m] = __builtin_elementwise_fma(d[c][m], (f32x2){wc[c], wc[c]}, s3[m]); }
            }
            float z[3][2];
#pragma unroll
            for (int e = 0; e < 2; ++e) {
                z[0][e] = row_sum(fold16(fold32(s1[0][e], s1[2][e]), fold32(s1[1][e], s1[3][e])));
                z[1][e] = row_sum(fold16(fold32(s2[0][e], s2[2][e]), fold32(s2[1][e], s2[3][e])));
                z[2][e] = row_sum(fold16(fold32(s3[0][e], s3[2][e]), fold32(s3[1][e], s3[3][e])));
            }
            const int li = lane & 15;
            if (li < 6) {
                const int e = li & 1, zi = li >> 1;
                const float v0 = zi == 0 ? z[0][0] : (zi == 1 ? z[1][0] : z[2][0]);
                const float v1 = zi == 0 ? z[0][1] : (zi == 1 ? z[1][1] : z[2][1]);
                *(LAS float*)(L + L_RED + (((2 * (lane >> 4) + e) * 3 + zi) * 4 + w) * 4) = e ? v1 : v0;
            }
        }
        lds_post(L, W_CB + w, 3);
        lds_wait4<false>(L, W_CB, 3);
        float md, inv2, rr;
        {
            const LAS f32x4* rp = (const LAS f32x4*)(L + L_RED + (lane & 7) * 48);
            const f32x4 a = rp[0], c = rp[1], e = rp[2];
            const float S1 = (a[0] + a[1]) + (a[2] + a[3]), S2 = (c[0] + c[1]) + (c[2] + c[3]), S3 = (e[0] + e[1]) + (e[2] + e[3]);
            const float mean = S1 * (1.f / DM);
            const float var = fmaxf(S2 * (1.f / DM) - mean * mean, 0.f);
            md = rb(piv + mean) - piv;
            inv2 = __builtin_amdgcn_rcpf(rb(__builtin_amdgcn_sqrtf(rb(rb(var) + 1e-5f))));
            rr = (S3 - md * sumw) * inv2;
            rr = rr == rr ? rr : -INFINITY;
        }
        float mx = fmaxf(rr, dppf<0x124>(rr)); mx = fmaxf(mx, dppf<0x122>(mx)); mx = fmaxf(mx, dppf<0x121>(mx));
        const int tb = __builtin_ctzll(__ballot(rr == mx)) & 7;
        if (tid == 0) __builtin_amdgcn_raw_buffer_store_b64((v2u){__float_as_uint(mx), (unsigned)(8 * G + tb)}, ws, OFF_CAND + G * 64, 0, SC1);
        v2u ocand;
        {
            float ds[4];
#pragma unroll
            for (int c = 0; c < 4; ++c) {
                const f32x8 dd = {d[c][0].x, d[c][0].y, d[c][1].x, d[c][1].y, d[c][2].x, d[c][2].y, d[c][3].x, d[c][3].y};
                ds[c] = dd[tb];
            }
            const float mdb = rdl(md, tb), ivb = rdl(inv2, tb), pvb = rdl(piv, tb);
            const unsigned n0 = nosent(pk((ds[0] - mdb) * ivb, (ds[1] - mdb) * ivb)), n1 = nosent(pk((ds[2] - mdb) * ivb, (ds[3] - mdb) * ivb));
            const unsigned h0 = nosent(pk(ds[0] + pvb, ds[1] + pvb)), h1 = nosent(pk(ds[2] + pvb, ds[3] + pvb));
            const bool odd = lane & 1;
            const unsigned e0 = dppu<0xB1>(odd ? n0 : h0), e1 = dppu<0xB1>(odd ? n1 : h1);
            const v4u o = odd ? (v4u){e0, e1, h0, h1} : (v4u){n0, n1, e0, e1};
            __builtin_amdgcn_raw_buffer_store_b128(o, ws, OFF_ROWS + G * 4096 + (odd ? 2048 : 0) + (t4 >> 1) * 16, 0, SC1);
            ocand = (v2u){pk(lo(h0) + lo(n0), hi(h0) + hi(n0)), pk(lo(h1) + lo(n1), hi(h1) + hi(n1))};
        }
        __builtin_amdgcn_s_setprio(0);
        STAMP(9);
        const rsrc_t ro = mk(p.out, SEQ * DM * 2);
        v4u ov[4];
        {
#pragma unroll
            for (int m = 0; m < 4; ++m) {
                const float i0 = rdl(inv2, 2 * m), i1 = rdl(inv2, 2 * m + 1);
                const f32x2 sc = {1.f + i0, 1.f + i1};
                const f32x2 of = {rdl(piv, 2 * m) - rdl(md, 2 * m) * i0, rdl(piv, 2 * m + 1) - rdl(md, 2 * m + 1) * i1};
                f32x2 o[4];
#pragma unroll
                for (int c = 0; c < 4; ++c) o[c] = __builtin_elementwise_fma(d[c][m], sc, of);
                const unsigned a0 = pk(o[0].x, o[1].x), a1 = pk(o[2].x, o[3].x), c0 = pk(o[0].y, o[1].y), c1 = pk(o[2].y, o[3].y);
                const bool odd = lane & 1;
                const unsigned e0 = dppu<0xB1>(odd ? a0 : c0), e1 = dppu<0xB1>(odd ? a1 : c1);
                ov[m] = odd ? (v4u){e0, e1, c0, c1} : (v4u){a0, a1, e0, e1};
            }
        }
#pragma unroll
        for (int m = 0; m < 4; ++m) { const int t = 2 * m + (lane & 1); if (t != tb) __builtin_amdgcn_raw_buffer_store_b128(ov[m], ro, (8 * G + t) * 2048 + (t4 >> 1) * 16, 0, OUT_AUX); }
        lds_wait4<true>(L, W_WIN, 1);
        int win; float rw;
        {
            const v4u wi = *(volatile LAS v4u*)(L + L_SYNC + 4 * W_WIN);
            const f32x4 wr4 = *(volatile LAS f32x4*)(L + L_SYNC + 4 * W_WR);
            rw = wr4[0]; unsigned bi = wi.x;
            if (wr4[1] > rw) { rw = wr4[1]; bi = wi.y; }
            if (wr4[2] > rw) { rw = wr4[2]; bi = wi.z; }
            if (wr4[3] > rw) { rw = wr4[3]; bi = wi.w; }
            win = __builtin_amdgcn_readfirstlane((bi - 1) & 2047);
        }
        if (win != 8 * G + tb) __builtin_amdgcn_raw_buffer_store_b64(ocand, ro, (8 * G + tb) * 2048 + t4 * 8, 0, OUT_AUX);
        STAMP(10);
        if (wave == 0) {
            const int col = lane >> 4;
            unsigned hn = SENT, hh = SENT;
#pragma nounroll
            for (unsigned s = 0; s < SPIN_G; ++s) {
                hn = __builtin_amdgcn_raw_buffer_load_b32(ws, OFF_ROWS + (win >> 3) * 4096 + G * 8 + (col >> 1) * 4, 0, SC1);
                hh = __builtin_amdgcn_raw_buffer_load_b32(ws, OFF_ROWS + (win >> 3) * 4096 + 2048 + G * 8 + (col >> 1) * 4, 0, SC1);
                if (hn != SENT && hh != SENT) break;
                CFENCE();
            }
            const float sg = rb(__builtin_amdgcn_rcpf(1.f + __expf(-rb(rw))));
            lds_wait4<true>(L, W_HB, 2);
            __builtin_amdgcn_s_setprio(3);
            int off[4]; v4u x[4];
#pragma unroll
            for (int i = 0; i < 4; ++i) { const int f = lane + 64 * i; off[i] = f < NFS ? OFF_PART + f * 4096 + G * 16 : (int)0x80000000; }
            poll16<4>(ws, off, x);
            float s[4];
#pragma unroll
            for (int c = 0; c < 4; ++c) s[c] = (__uint_as_float(x[0][c]) + __uint_as_float(x[1][c])) + (__uint_as_float(x[2][c]) + __uint_as_float(x[3][c]));
            const float tot = row_sum(fold16(fold32(s[0], s[2]), fold32(s[1], s[3])));
            const float yv = rb(rb(tot) * sg);
            const float n = (col & 1) ? hi(hn) : lo(hn), h = (col & 1) ? hi(hh) : lo(hh);
            if ((lane & 15) == 0) __builtin_amdgcn_raw_buffer_store_b16((u16)pk(h + rb(n + yv), 0.f), ro, win * 2048 + G * 8 + col * 2, 0, SC1);
        }
        STAMP(11);
    } else {
        unsigned junk, junk1, junk2 = 0;
        {
            const rsrc_t rc = mk((const void*)(pc0 & ~255ull), CODE_BYTES);
            junk = __builtin_amdgcn_raw_buffer_load_b32(rc, t4 * 64, 0, 0);
            junk1 = __builtin_amdgcn_raw_buffer_load_b32(rc, t4 * 64 + 16384, 0, 0);
        }
        if (wave == 4) *(LAS unsigned*)(L + L_SYNC + lane * 4) = 0u;
        asm volatile("s_waitcnt lgkmcnt(0)" ::: "memory");
        __builtin_amdgcn_s_barrier();
        CFENCE();
        const rsrc_t ws = mk(p.ws, WS_USED);
        const int chc = 16 * (G >> 2) + (G & 3) + 4 * w;
        const int fs = (G & 31) < 28 ? (G >> 5) * 28 + (G & 31) : -1;
        const v4u ss = {SENT, SENT, SENT, SENT};
#pragma unroll
        for (int c = 0; c < 4; ++c) __builtin_amdgcn_raw_buffer_store_b128(ss, ws, OFF_X1 + G * 16384 + c * 4096 + t4 * 16, 0, SC1);
        const v2u ep = __builtin_amdgcn_raw_buffer_load_b64(ws, OFF_EPOCH, 0, SC1);
#pragma unroll
        for (int c = 0; c < 4; ++c) __builtin_amdgcn_raw_buffer_store_b128(ss, ws, OFF_X2 + G * 16384 + c * 4096 + t4 * 16, 0, SC1);
        __builtin_amdgcn_raw_buffer_store_b128(ss, ws, OFF_ROWS + G * 4096 + t4 * 16, 0, SC1);
        if (fs >= 0) __builtin_amdgcn_raw_buffer_store_b128(ss, ws, OFF_PART + fs * 4096 + t4 * 16, 0, SC1);
        if (t4 < 4) __builtin_amdgcn_raw_buffer_store_b128(ss, ws, OFF_CAND + G * 64 + t4 * 16, 0, SC1);
        __builtin_amdgcn_sched_barrier(0);
        const rsrc_t rh = mk(p.hk + chc * SEQ, SEQ * 2);
        v4u ha[5], hb[5];
#pragma unroll
        for (int i = 0; i < 5; ++i) {
            const int c = lane + 64 * i;
            ha[i] = __builtin_amdgcn_raw_buffer_load_b128(rh, c >= 2 ? c * 16 - 32 : (int)0x80000000, 0, 0);
            hb[i] = __builtin_amdgcn_raw_buffer_load_b128(rh, c >= 1 ? c * 16 - 16 : (int)0x80000000, 0, 0);
        }
        const GAS v4u* qp = (const GAS v4u*)(p.q + (8 * G + 2 * w) * DM);
        const v4u q0 = qp[lane], q1 = qp[64 + lane], q2 = qp[128 + lane], q3 = qp[192 + lane];
        const v4u w0 = ((const GAS v4u*)p.wr)[lane], w1 = ((const GAS v4u*)p.wr)[64 + lane];
        {
            const unsigned char* tp = lane < 12 ? p.ws + (lane << 20) : lane < 16 ? (const unsigned char*)p.out + ((lane - 12) << 20)
                                    : lane < 23 ? (const unsigned char*)p.wg + ((lane - 16) << 20) : lane < 30 ? (const unsigned char*)p.wu + ((lane - 23) << 20)
                                    : lane < 37 ? (const unsigned char*)p.wd + ((lane - 30) << 20) : (const unsigned char*)p.dv;
            if (w == 3) junk2 = *(const GAS unsigned*)(tp + (lane < 37 ? 1048448 : 0));
        }
        __builtin_amdgcn_sched_barrier(0);
        asm volatile("s_waitcnt vmcnt(17)" ::: "memory");
        asm volatile("s_waitcnt vmcnt(16)" ::: "memory");
        lds_post(L, W_CLR + w, 1);
        STAMP(1);
        const u64 tag = (((u64)ep.y << 32) | ep.x) + 1;
        const v2u tagv = {(unsigned)tag, (unsigned)(tag >> 32)};
        if (w == 0) {
            lds_wait4<false>(L, W_CLR, 1);
            if (lane == 0) __builtin_amdgcn_raw_buffer_store_b64(tagv, ws, OFF_FL + G * 64, 0, SC1);
        }
        {
            LAS unsigned char* hp = L + w * HC_CH + lane * 16;
#pragma unroll
            for (int i = 0; i < 5; ++i) {
                const unsigned e[8] = {ha[i].x, ha[i].y, ha[i].z, ha[i].w, hb[i].x, hb[i].y, hb[i].z, hb[i].w};
                unsigned o[7];
#pragma unroll
                for (int j = 0; j < 7; ++j) o[j] = __builtin_amdgcn_alignbit(e[j + 1], e[j], 16);
                if (i < 4 || lane < 4) {
#pragma unroll
                    for (int a = 0; a < 4; ++a) {
                        *(LAS v4u*)(hp + (2 * a) * HC_COPY + i * 1024) = (v4u){o[a], o[a + 1], o[a + 2], o[a + 3]};
                        *(LAS v4u*)(hp + (2 * a + 1) * HC_COPY + i * 1024) = (v4u){e[a + 1], e[a + 2], e[a + 3], e[a + 4]};
                    }
                }
            }
        }
        STAMP(2);
#pragma nounroll
        for (unsigned s = 0; s < SPIN_G; ++s) {
            const v2u f = __builtin_amdgcn_raw_buffer_load_b64(ws, OFF_FL + t4 * 64, 0, SC1);
            if (f.x == tagv.x && f.y == tagv.y) break;
            CFENCE();
        }
        lds_post(L, W_OK + w, 1);
        STAMP(3);
        {
            float mv[2], iv[2];
#pragma unroll
            for (int r = 0; r < 2; ++r) {
                const v4u a = r ? q2 : q0, c = r ? q3 : q1;
                float x[16];
#pragma unroll
                for (int j = 0; j < 4; ++j) { x[2 * j] = lo(a[j]); x[2 * j + 1] = hi(a[j]); x[8 + 2 * j] = lo(c[j]); x[9 + 2 * j] = hi(c[j]); }
                float s = 0.f;
#pragma unroll
                for (int j = 0; j < 16; ++j) s += x[j];
                const float mean = wave_sum(s) * (1.f / DM);
                float vv = 0.f;
#pragma unroll
                for (int j = 0; j < 16; ++j) vv += (x[j] - mean) * (x[j] - mean);
                const float var = wave_sum(vv) * (1.f / DM);
                mv[r] = rb(mean);
                iv[r] = __builtin_amdgcn_rcpf(rb(__builtin_amdgcn_sqrtf(rb(rb(var) + 1e-5f))));
            }
            float s = 0.f;
#pragma unroll
            for (int j = 0; j < 4; ++j) s += (lo(w0[j]) + hi(w0[j])) + (lo(w1[j]) + hi(w1[j]));
            s = wave_sum(s);
            if (lane == 0) {
                *(LAS f32x4*)(L + L_ST1 + w * 16) = (f32x4){mv[0], iv[0], mv[1], iv[1]};
                if (w == 0) *(LAS float*)(L + L_ST1 + 64) = s;
            }
        }
        lds_post(L, W_HC + w, 1);
        asm volatile("" :: "v"(junk), "v"(junk1), "v"(junk2));
        lds_wait4<true>(L, W_CB, 1);
        STAMP(4);
        v4u bg[8], bu[8];
        unsigned wdp[8][4];
        if (fs >= 0) {
            const rsrc_t rg_ = mk(p.wg, DM * FF * 2), ru_ = mk(p.wu, DM * FF * 2), rd_ = mk(p.wd, DM * FF * 2);
            const int go = (256 * w + (lane >> 1)) * (FF * 2) + fs * 32 + (lane & 1) * 16;
            v4u g_[8], u_[8]; v2u dn[16];
#pragma unroll
            for (int i = 0; i < 8; ++i) {
                g_[i] = __builtin_amdgcn_raw_buffer_load_b128(rg_, go, i * 32 * FF * 2, WGU_AUX);
                u_[i] = __builtin_amdgcn_raw_buffer_load_b128(ru_, go, i * 32 * FF * 2, WGU_AUX);
            }
#pragma unroll
            for (int i = 0; i < 16; ++i) dn[i] = __builtin_amdgcn_raw_buffer_load_b64(rd_, fs * 16 * 2048 + t4 * 8, i * 2048, NT);
            LAS unsigned char* tp = L + L_TR + w * 1024;
            const LAS unsigned char* tr = tp + (8 * (lane >> 4) + ((lane & 15) >> 2)) * 32 + (lane & 3) * 8;
#pragma unroll
            for (int i = 0; i < 16; ++i) {
                *(LAS v4u*)(tp + lane * 16) = (i & 1) ? u_[i >> 1] : g_[i >> 1];
                CFENCE();
                const v2u a = __builtin_bit_cast(v2u, __builtin_amdgcn_ds_read_tr16_b64_v4i16((LAS s16x4*)tr));
                const v2u c = __builtin_bit_cast(v2u, __builtin_amdgcn_ds_read_tr16_b64_v4i16((LAS s16x4*)(tr + 128)));
                CFENCE();
                if (i & 1) bu[i >> 1] = (v4u){a.x, a.y, c.x, c.y}; else bg[i >> 1] = (v4u){a.x, a.y, c.x, c.y};
            }
#pragma unroll
            for (int m = 0; m < 8; ++m) {
                wdp[m][0] = __builtin_amdgcn_perm(dn[2 * m + 1].x, dn[2 * m].x, 0x05040100);
                wdp[m][1] = __builtin_amdgcn_perm(dn[2 * m + 1].x, dn[2 * m].x, 0x07060302);
                wdp[m][2] = __builtin_amdgcn_perm(dn[2 * m + 1].y, dn[2 * m].y, 0x05040100);
                wdp[m][3] = __builtin_amdgcn_perm(dn[2 * m + 1].y, dn[2 * m].y, 0x07060302);
            }
        }
        STAMP(5);
        lds_wait4<true>(L, W_CB, 3);
        if (G == 0 && tid == 256) __builtin_amdgcn_raw_buffer_store_b64(tagv, ws, OFF_EPOCH, 0, SC1);
        __builtin_amdgcn_s_setprio(3);
        STAMP(6);
        int win;
        {
            v2u c = {SENT, SENT};
#pragma nounroll
            for (unsigned s = 0; s < SPIN_G; ++s) {
                c = __builtin_amdgcn_raw_buffer_load_b64(ws, OFF_CAND + t4 * 64, 0, SC1);
                if (c.x != SENT && c.y != SENT) break;
                CFENCE();
            }
            const float br = __uint_as_float(c.x);
            const float mx = wave_max(br);
            const unsigned bw = __builtin_amdgcn_readlane(c.y, __builtin_ctzll(__ballot(br == mx)) & 63);
            lds_post(L, W_WR + w, __float_as_uint(mx));
            lds_post(L, W_WIN + w, (bw & 2047) + 1);
            lds_wait4<false>(L, W_WIN, 1);
            const v4u wi = *(volatile LAS v4u*)(L + L_SYNC + 4 * W_WIN);
            const f32x4 wr4 = *(volatile LAS f32x4*)(L + L_SYNC + 4 * W_WR);
            float rw = wr4[0]; unsigned bi = wi.x;
            if (wr4[1] > rw) { rw = wr4[1]; bi = wi.y; }
            if (wr4[2] > rw) { rw = wr4[2]; bi = wi.z; }
            if (wr4[3] > rw) { rw = wr4[3]; bi = wi.w; }
            win = __builtin_amdgcn_readfirstlane((bi - 1) & 2047);
        }
        STAMP(7);
        if (fs >= 0) {
            int xo[8]; v4u xa[8];
#pragma unroll
            for (int i = 0; i < 8; ++i) xo[i] = OFF_ROWS + (win >> 3) * 4096 + (256 * w + 32 * i + 8 * (lane >> 4)) * 2;
            poll16<8>(ws, xo, xa);
            STAMP(8);
            f32x4 ag = {0.f, 0.f, 0.f, 0.f}, au = {0.f, 0.f, 0.f, 0.f};
#pragma unroll
            for (int i = 0; i < 8; ++i) { ag = mfma(xa[i], bg[i], ag); au = mfma(xa[i], bu[i], au); }
            if (lane < 32) *(LAS float*)(L + L_GU + lane * 16 + w * 4) = lane < 16 ? ag[0] : au[0];
            lds_post(L, W_HB + w, 1);
            lds_wait4<false>(L, W_HB, 1);
            const f32x4 pg = *(const LAS f32x4*)(L + L_GU + (lane & 15) * 16), pu = *(const LAS f32x4*)(L + L_GU + 256 + (lane & 15) * 16);
            const float g = rb((pg[0] + pg[1]) + (pg[2] + pg[3])), u = rb((pu[0] + pu[1]) + (pu[2] + pu[3]));
            const float a = rb(g * __builtin_amdgcn_rcpf(1.f + __expf(-g))) * u;
            const unsigned ap = pk(a, dppf<0xB1>(a));
            float o[4] = {0.f, 0.f, 0.f, 0.f};
#pragma unroll
            for (int m = 0; m < 8; ++m) {
                const unsigned am = __builtin_amdgcn_readlane(ap, 2 * m);
#pragma unroll
                for (int c2 = 0; c2 < 4; ++c2)
                    o[c2] = __builtin_amdgcn_fdot2_f32_bf16(__builtin_bit_cast(bf16x2, wdp[m][c2]), __builtin_bit_cast(bf16x2, am), o[c2], false);
            }
            __builtin_amdgcn_raw_buffer_store_b128((v4u){nosent(__float_as_uint(o[0])), nosent(__float_as_uint(o[1])), nosent(__float_as_uint(o[2])), nosent(__float_as_uint(o[3]))},
                                                   ws, OFF_PART + fs * 4096 + t4 * 16, 0, SC1);
        }
        lds_post(L, W_HB + w, 2);
        STAMP(9);
    }
}

extern "C" void kernel_launch(void* const* d_in, const int* in_sizes, int n_in, void* d_out, int out_size, void* d_ws, size_t ws_size,
                              hipStream_t stream) {
    Params p;
    p.q = (const u16*)d_in[0]; p.k = (const u16*)d_in[1]; p.v = (const u16*)d_in[2]; p.sk = (const u16*)d_in[3];
    p.hk = (const u16*)d_in[4]; p.dv = (const u16*)d_in[5]; p.wr = (const u16*)d_in[7]; p.wg = (const u16*)d_in[8];
    p.wu = (const u16*)d_in[9]; p.wd = (const u16*)d_in[10];
    p.out = (u16*)d_out; p.ws = (unsigned char*)d_ws + WS_BASE;
    (void)hipFuncSetAttribute((const void*)h3_mod_kernel, hipFuncAttributeMaxDynamicSharedMemorySize, LDS_BYTES);
    h3_mod_kernel<<<NWG, NTHR, LDS_BYTES, stream>>>(p);
}
```

```cpp
#include <hip/hip_runtime.h>
#include <hip/hip_bf16.h>

#define GAS __attribute__((address_space(1)))
#define LAS __attribute__((address_space(3)))
typedef unsigned short u16;
typedef unsigned long long u64;
typedef unsigned v4u __attribute__((ext_vector_type(4)));
typedef unsigned v2u __attribute__((ext_vector_type(2)));
typedef float f32x4 __attribute__((ext_vector_type(4)));
typedef float f32x2 __attribute__((ext_vector_type(2)));
typedef float f32x8 __attribute__((ext_vector_type(8)));
typedef __bf16 bf16x2 __attribute__((ext_vector_type(2)));
typedef __bf16 bf16x8 __attribute__((ext_vector_type(8)));
typedef short s16x4 __attribute__((ext_vector_type(4)));
typedef __amdgpu_buffer_rsrc_t rsrc_t;

constexpr int SEQ = 2048, DM = 1024, FF = 3584;
constexpr int NWG = 256, NTHR = 512;
constexpr int SC1 = 16;
constexpr int NT = 2;
constexpr int OOB = (int)0x80000000;

constexpr int OFF_EPOCH = 0;
constexpr int OFF_CAND = 16384;
constexpr int OFF_H = 32768;
constexpr int OFF_F1 = 65536;
constexpr int OFF_MID = 1 << 19;
constexpr int OFF_PART = 1 << 20;
constexpr int OFF_ROWS = 2 << 20;
constexpr int OFF_X1 = 4 << 20;
constexpr int OFF_X2 = 8 << 20;
#define WS_BASE 0
#define CODE_BYTES 16384
#define PT 0
#define CODE_EARLY 1
#define WGU_AUX NT
#define H_AUX NT
#define OUT_AUX SC1
#define NB0 0
#define NB1 0
#define NB2 8
#define NB3 16
#define NB4 24
constexpr int OFF_DBG = 16 << 20;
constexpr int WS_USED = 32 << 20;
constexpr int T1 = 5;

constexpr int HC_COPY = 260 * 16;
constexpr int HC_CH = 8 * HC_COPY;
constexpr int XB_OFF = 4 * HC_CH;
constexpr int XB_CH = 286 * 16;
constexpr int L_SYNC = XB_OFF + 4 * XB_CH;
constexpr int L_STG = L_SYNC + 256;
constexpr int LDS_BYTES = L_STG + T1 * 2048;
constexpr int L_STG2 = 0;
constexpr int L_RED = 8192;
constexpr int L_GU = 10240;
constexpr int L_TR = 12288;
constexpr int W_HC = 0, W_CB = 4, W_HB = 8, W_WIN = 12, W_WR = 16, W_TAG = 20, W_XI = 24;

constexpr unsigned SPIN_G = 1u << 18, SPIN_L = 1u << 22;

#define STAMP(i) do {} while (0)

struct Params {
    const u16 *q, *k, *v, *sk, *hk, *dv, *wr, *wg, *wu, *wd;
    u16* out;
    unsigned char* ws;
};

__device__ __forceinline__ rsrc_t mk(const void* p, unsigned bytes) { return __builtin_amdgcn_make_buffer_rsrc((void*)p, 0, bytes, 0x00020000); }
__device__ __forceinline__ float lo(unsigned x) { return __uint_as_float(x << 16); }
__device__ __forceinline__ float hi(unsigned x) { return __uint_as_float(x & 0xFFFF0000u); }
__device__ __forceinline__ unsigned pk(float a, float b) { f32x2 t = {a, b}; return __builtin_bit_cast(unsigned, __builtin_convertvector(t, bf16x2)); }
__device__ __forceinline__ float rb(float a) { return lo(pk(a, a) & 0xFFFFu); }
__device__ __forceinline__ f32x4 mfma(v4u a, v4u b, f32x4 c) {
    return __builtin_amdgcn_mfma_f32_16x16x32_bf16(__builtin_bit_cast(bf16x8, a), __builtin_bit_cast(bf16x8, b), c, 0, 0, 0);
}
template <int CTRL> __device__ __forceinline__ float dppf(float x) {
    return __uint_as_float(__builtin_amdgcn_update_dpp(0u, __float_as_uint(x), CTRL, 0xF, 0xF, true));
}
template <int CTRL> __device__ __forceinline__ unsigned dppu(unsigned x) { return __builtin_amdgcn_update_dpp(0u, x, CTRL, 0xF, 0xF, true); }
__device__ __forceinline__ float row_sum(float x) {
    x += dppf<0x128>(x); x += dppf<0x124>(x); x += dppf<0x122>(x); x += dppf<0x121>(x);
    return x;
}
__device__ __forceinline__ float fold32(float a, float b) {
    auto r = __builtin_amdgcn_permlane32_swap(__float_as_uint(a), __float_as_uint(b), false, false);
    return __uint_as_float(r[0]) + __uint_as_float(r[1]);
}
__device__ __forceinline__ float fold16(float a, float b) {
    auto r = __builtin_amdgcn_permlane16_swap(__float_as_uint(a), __float_as_uint(b), false, false);
    return __uint_as_float(r[0]) + __uint_as_float(r[1]);
}
__device__ __forceinline__ float fold8(float a, float b, bool up) {
    const float keep = up ? b : a, give = up ? a : b;
    return keep + dppf<0x128>(give);
}
__device__ __forceinline__ float wave_max(float x) {
    auto r = __builtin_amdgcn_permlane32_swap(__float_as_uint(x), __float_as_uint(x), false, false);
    x = fmaxf(__uint_as_float(r[0]), __uint_as_float(r[1]));
    auto s = __builtin_amdgcn_permlane16_swap(__float_as_uint(x), __float_as_uint(x), false, false);
    x = fmaxf(__uint_as_float(s[0]), __uint_as_float(s[1]));
    x = fmaxf(x, dppf<0x128>(x)); x = fmaxf(x, dppf<0x124>(x)); x = fmaxf(x, dppf<0x122>(x)); x = fmaxf(x, dppf<0x121>(x));
    return x;
}
__device__ __forceinline__ float rdl(float x, int l) { return __uint_as_float(__builtin_amdgcn_readlane(__float_as_uint(x), l)); }
__device__ __forceinline__ u64 tagof(v4u x) { return ((u64)x.w << 32) | x.z; }

#define CFENCE() asm volatile("" ::: "memory")
#define PIN(x) asm volatile("" : "+v"(x))
__device__ __forceinline__ void lds_post(LAS unsigned char* L, int word, unsigned val) {
    CFENCE();
    *(volatile LAS unsigned*)(L + L_SYNC + 4 * word) = val;
    CFENCE();
}
template <bool SLEEP> __device__ __forceinline__ void lds_wait4(LAS unsigned char* L, int group, unsigned val) {
#pragma nounroll
    for (unsigned s = 0; s < SPIN_L; ++s) {
        const v4u x = *(volatile LAS v4u*)(L + L_SYNC + 4 * group);
        const unsigned m = __builtin_amdgcn_readfirstlane(min(min(x.x, x.y), min(x.z, x.w)));
        if (m >= val) break;
        if (SLEEP) __builtin_amdgcn_s_sleep(1);
    }
    CFENCE();
}
template <bool SLEEP> __device__ __forceinline__ void lds_wait1(LAS unsigned char* L, int word, unsigned val) {
#pragma nounroll
    for (unsigned s = 0; s < SPIN_L; ++s) {
        const unsigned m = __builtin_amdgcn_readfirstlane(*(volatile LAS unsigned*)(L + L_SYNC + 4 * word));
        if (m >= val) break;
        if (SLEEP) __builtin_amdgcn_s_sleep(1);
    }
    CFENCE();
}
__device__ __forceinline__ u64 lds_tag(LAS unsigned char* L) {
    v4u x;
#pragma nounroll
    for (unsigned s = 0; s < SPIN_L; ++s) {
        x = *(volatile LAS v4u*)(L + L_SYNC + 4 * W_TAG);
        if (__builtin_amdgcn_readfirstlane(x.z)) break;
    }
    CFENCE();
    return ((u64)__builtin_amdgcn_readfirstlane(x.y) << 32) | __builtin_amdgcn_readfirstlane(x.x);
}
template <int N, bool T32> __device__ __forceinline__ void pollg(rsrc_t rs, const int (&off)[N], v4u (&x)[N], u64 tag) {
#pragma nounroll
    for (unsigned s = 0; s < SPIN_G; ++s) {
        bool bad = false;
#pragma unroll
        for (int i = 0; i < N; ++i) x[i] = __builtin_amdgcn_raw_buffer_load_b128(rs, off[i], 0, SC1);
#pragma unroll
        for (int i = 0; i < N; ++i) bad |= T32 ? x[i].w != (unsigned)tag : tagof(x[i]) != tag;
        if (!__builtin_amdgcn_ballot_w64(bad)) break;
        CFENCE();
    }
    CFENCE();
}
template <int N> __device__ __forceinline__ void pollm(rsrc_t rs, const int (&off)[N], v4u (&x)[N], u64 tag) {
#pragma unroll
    for (int i = 0; i < N; ++i) x[i] = __builtin_amdgcn_raw_buffer_load_b128(rs, off[i], 0, SC1);
#pragma nounroll
    for (unsigned s = 0; s < SPIN_G; ++s) {
        bool bad = false;
#pragma unroll
        for (int i = 0; i < N; ++i) bad |= tagof(x[i]) != tag;
        if (!__builtin_amdgcn_ballot_w64(bad)) break;
        CFENCE();
#pragma unroll
        for (int i = 0; i < N; ++i)
            if (tagof(x[i]) != tag) x[i] = __builtin_amdgcn_raw_buffer_load_b128(rs, off[i], 0, SC1);
    }
    CFENCE();
}
template <int D> __device__ __forceinline__ void spread(unsigned a, unsigned c, v4u& A0, v4u& A1) {
    A0 = (v4u){dppu<0x150>(a), dppu<0x150 + D>(c), dppu<0x158>(a), dppu<0x158 + D>(c)};
    A1 = (v4u){dppu<0x154>(a), dppu<0x154 + D>(c), dppu<0x15C>(a), dppu<0x15C + D>(c)};
}
template <int NZ> __device__ __forceinline__ void row_partials(LAS unsigned char* dst, const f32x2 (&s)[3][4], int lane, int w) {
    const bool up = lane & 8;
    float z[NZ];
#pragma unroll
    for (int i = 0; i < NZ; ++i) {
        const float e0 = fold16(fold32(s[i][0].x, s[i][2].x), fold32(s[i][1].x, s[i][3].x));
        const float e1 = fold16(fold32(s[i][0].y, s[i][2].y), fold32(s[i][1].y, s[i][3].y));
        float t = fold8(e0, e1, up);
        t += dppf<0x141>(t);
        t += dppf<0x4E>(t); t += dppf<0xB1>(t);
        z[i] = t;
    }
    const int li = lane & 7;
    if (li < NZ) *(LAS float*)(dst + (((lane >> 3) * 3 + li) * 4 + w) * 4) = li == 0 ? z[0] : (li == 1 ? z[1] : z[NZ - 1]);
}
__device__ __forceinline__ float sum4(f32x4 a) { return (a[0] + a[1]) + (a[2] + a[3]); }
__device__ __forceinline__ float inv_std(float var) { return __builtin_amdgcn_rcpf(rb(__builtin_amdgcn_sqrtf(rb(rb(var) + 1e-5f)))); }

__global__ void __launch_bounds__(NTHR, 2) h3_mod_kernel(Params p) {
    extern __shared__ __attribute__((aligned(16))) unsigned char smem[];
    LAS unsigned char* L = (LAS unsigned char*)smem;
    u64 pc0;
    asm volatile("s_getpc_b64 %0" : "=s"(pc0));
    const int tid = threadIdx.x, lane = tid & 63;
    const int wave = __builtin_amdgcn_readfirstlane(tid >> 6), w = wave & 3;
    const int b = blockIdx.x, x = b & 7, j = b >> 3;
    const int t4 = 64 * w + lane;
    const int chc = 128 * x + 16 * (j >> 2) + (j & 3) + 4 * w;
    STAMP(0);
    STAMP(14);

    if (__builtin_expect(wave < 4, 1)) {
        const rsrc_t rk = mk(p.k, SEQ * DM * 2), rv = mk(p.v, SEQ * DM * 2);
        const int cg = lane & 31, rg = 2 * w + (lane >> 5);
        const int vo = (64 * j + 8 * rg) * 2048 + x * 256 + cg * 8;
        const int vh = vo >= 3 * 2048 ? vo - 3 * 2048 : OOB;
        v2u kr[11], vr[8], qr[8];
#pragma unroll
        for (int i = 0; i < 8; ++i) {
            kr[3 + i] = __builtin_amdgcn_raw_buffer_load_b64(rk, vo, i * 2048, NT);
            vr[i] = __builtin_amdgcn_raw_buffer_load_b64(rv, vo, i * 2048, NT);
        }
#pragma unroll
        for (int i = 0; i < 3; ++i) kr[i] = __builtin_amdgcn_raw_buffer_load_b64(rk, vh, i * 2048, NT);
        const v4u sa = *(const GAS v4u*)(p.sk + (32 * x + cg) * 16), sb = *(const GAS v4u*)(p.sk + (32 * x + cg) * 16 + 8);
        __builtin_amdgcn_sched_barrier(0);
        STAMP(1);
        __builtin_amdgcn_s_setprio(2);
        const rsrc_t ws = mk(p.ws, WS_USED);
        const unsigned dbits = p.dv[chc];
        if (lane < 30) *(LAS v4u*)(L + XB_OFF + w * XB_CH + lane * 16) = (v4u){0u, 0u, 0u, 0u};
        asm volatile("s_waitcnt lgkmcnt(0)" ::: "memory");
        __builtin_amdgcn_s_barrier();
        CFENCE();
        __builtin_amdgcn_sched_barrier(0);
        {
            v4u kv[4];
            const f32x2 w01[4] = {{lo(sa.x), lo(sa.z)}, {hi(sa.x), hi(sa.z)}, {lo(sa.y), lo(sa.w)}, {hi(sa.y), hi(sa.w)}};
            const f32x2 w23[4] = {{lo(sb.x), lo(sb.z)}, {hi(sb.x), hi(sb.z)}, {lo(sb.y), lo(sb.w)}, {hi(sb.y), hi(sb.w)}};
            f32x2 ka[11], kb[11], xa[8], xb[8];
#pragma unroll
            for (int i = 0; i < 11; ++i) { ka[i] = (f32x2){lo(kr[i].x), hi(kr[i].x)}; kb[i] = (f32x2){lo(kr[i].y), hi(kr[i].y)}; }
#pragma unroll
            for (int t = 0; t < 8; ++t) {
                f32x2 a = ka[t] * w01[3], c = kb[t] * w23[3];
#pragma unroll
                for (int jj = 2; jj >= 0; --jj) { a = __builtin_elementwise_fma(ka[t + 3 - jj], w01[jj], a); c = __builtin_elementwise_fma(kb[t + 3 - jj], w23[jj], c); }
                xa[t] = a * (f32x2){lo(vr[t].x), hi(vr[t].x)};
                xb[t] = c * (f32x2){lo(vr[t].y), hi(vr[t].y)};
            }
#pragma unroll
            for (int m = 0; m < 4; ++m) {
                kv[0][m] = pk(xa[7 - 2 * m].x, xa[6 - 2 * m].x);
                kv[1][m] = pk(xa[7 - 2 * m].y, xa[6 - 2 * m].y);
                kv[2][m] = pk(xb[7 - 2 * m].x, xb[6 - 2 * m].x);
                kv[3][m] = pk(xb[7 - 2 * m].y, xb[6 - 2 * m].y);
            }
#pragma unroll
            for (int c = 0; c < 4; ++c)
                __builtin_amdgcn_raw_buffer_store_b128(kv[c], ws, OFF_X1 + (32 * x + j) * 16384 + (((cg & 28) + c + j) & 31) * 512 + rg * 64 + (cg & 3) * 16, 0, SC1);
        }
        STAMP(2);
        lds_post(L, W_CB + w, 1);
        const u64 tag = lds_tag(L);
        const unsigned tl = (unsigned)tag, th = (unsigned)(tag >> 32);
        asm volatile("s_waitcnt vmcnt(0)" ::: "memory");
        if (lane == 0) __builtin_amdgcn_raw_buffer_store_b64((v2u){tl, th}, ws, OFF_F1 + ((32 * x + j) * 4 + w) * 64, 0, SC1);
        STAMP(3);
#pragma nounroll
        for (unsigned s = 0; s < SPIN_G; ++s) {
            const v2u f = __builtin_amdgcn_raw_buffer_load_b64(ws, OFF_F1 + (32 * x + 8 * w) * 256 + (lane & 31) * 64, 0, SC1);
            if (!__builtin_amdgcn_ballot_w64(f.x != tl || f.y != th)) break;
            CFENCE();
        }
        CFENCE();
        STAMP(4);
        v2u wrr; unsigned qpr;
        {
            v4u xx[4];
#pragma unroll
            for (int i = 0; i < 4; ++i) {
                const int jp = 8 * w + 2 * i + (lane >> 5);
                xx[i] = __builtin_amdgcn_raw_buffer_load_b128(ws, OFF_X1 + (32 * x + jp) * 16384 + ((j + jp) & 31) * 512 + (lane & 31) * 16, 0, SC1);
            }
            __builtin_amdgcn_sched_barrier(0);
            lds_post(L, W_XI + w, 1);
            const rsrc_t rq = mk(p.q, SEQ * DM * 2);
            lds_wait4<false>(L, W_XI, 1);
#pragma unroll
            for (int i = 0; i < 8; ++i) qr[i] = __builtin_amdgcn_raw_buffer_load_b64(rq, b * 16384 + t4 * 8, i * 2048, NT);
            wrr = *(const GAS v2u*)(p.wr + t4 * 4);
            qpr = *(const GAS unsigned*)(p.q + (8 * b + (lane & 7)) * DM);
            __builtin_amdgcn_sched_barrier(0);
#pragma unroll
            for (int i = 0; i < 4; ++i) *(LAS v4u*)(L + XB_OFF + (lane & 3) * XB_CH + (30 + 64 * w + 16 * i + (lane >> 2)) * 16) = xx[i];
        }
        lds_post(L, W_CB + w, 2);
        STAMP(5);
        lds_wait1<false>(L, W_HC + w, 1);
        {
            const int i = lane & 15, g = lane >> 4;
            unsigned ap = w * HC_CH + (i & 7) * HC_COPY + (3 - g + (i >> 3)) * 16;
            unsigned bp = XB_OFF + w * XB_CH + (28 + 2 * i + g) * 16;
            unsigned st = L_STG + i * 128 + ((g * 32 + w * 8 + i * 8) & 127);
            unsigned st2 = w * HC_CH + w * 32 + g * 136 + i * 8;
#define LDA(u, o) (*(const LAS v4u*)(L + ap + 512 * (u) + (o)))
#define LDB(u) (*(const LAS v4u*)(L + bp + 512 * (u)))
            PIN(ap); PIN(bp); PIN(st); PIN(st2);
            v4u A[8], B[8];
            f32x4 acc[8];
            v4u AD;
            {
                const int jd = 8 * g - 9 - i;
                const unsigned dd = (jd & 1) ? dbits << 16 : dbits;
#pragma unroll
                for (int e = 0; e < 4; ++e) AD[e] = (jd >> 1) == e ? dd : 0u;
                PIN(AD);
            }
#pragma unroll
            for (int u = 0; u < 8; ++u) { A[u] = LDA(u, 0); PIN(A[u]); }
            lds_wait4<false>(L, W_CB, 2);
            STAMP(6);
#pragma unroll
            for (int u = 0; u < 8; ++u) B[u] = LDB(u);
#pragma unroll
            for (int n = 0; n < 8; ++n) acc[n] = mfma(AD, B[n], (f32x4){0.f, 0.f, 0.f, 0.f});
            __builtin_amdgcn_sched_barrier(0);
#define MM(m, u) acc[(m) + (u)] = mfma(A[m], B[u], acc[(m) + (u)]); __builtin_amdgcn_sched_barrier(0)
#define RA(m) A[m] = LDA(m, 0); __builtin_amdgcn_sched_barrier(0)
#define RB(u) B[u] = LDB(u); __builtin_amdgcn_sched_barrier(0)
#pragma unroll 1
            for (int r = 0; r < 8; ++r) {
                bp = r < 7 ? bp - 64 : bp + 448;
                ap = r < 7 ? ap + 64 : ap - 448;
                PIN(bp); PIN(ap);
                __builtin_amdgcn_sched_barrier(0);
                MM(0, 4); RB(4); MM(0, 3); MM(0, 2); MM(1, 3); RB(3); MM(0, 1); MM(1, 2); MM(0, 0); RA(0); MM(1, 1); MM(2, 2); RB(2);
                MM(1, 0); RA(1); MM(2, 1); MM(3, 1); RB(1); MM(2, 0); RA(2); MM(3, 0); RA(3); MM(4, 0); RA(4); RB(0);
            }
#pragma unroll
            for (int n = 0; n < T1; ++n)
                *(LAS v2u*)(L + st + n * 2048) = (v2u){pk(acc[n][0], acc[n][1]), pk(acc[n][2], acc[n][3])};
            lds_post(L, W_CB + w, 3);
            STAMP(7);
#pragma unroll 1
            for (int r = 0; r < 8; ++r) {
                bp -= 64; ap += 64;
                PIN(bp); PIN(ap);
                __builtin_amdgcn_sched_barrier(0);
                MM(0, 7); RB(7); MM(0, 6); MM(0, 5); RA(0);
                MM(1, 6); RB(6); MM(1, 5); MM(1, 4); RA(1);
                MM(2, 5); RB(5); MM(2, 4); MM(2, 3); RA(2);
                MM(3, 4); RB(4); MM(3, 3); MM(3, 2); RA(3);
                MM(4, 3); RB(3); MM(4, 2); MM(4, 1); RA(4);
                MM(5, 2); RB(2); MM(5, 1); MM(5, 0); RA(5);
                MM(6, 1); RB(1); MM(6, 0); RA(6);
                MM(7, 0); RB(0); RA(7);
            }
#pragma unroll
            for (int n = T1; n < 8; ++n)
                *(LAS v2u*)(L + st2 + (n - T1) * 544) = (v2u){pk(acc[n][0], acc[n][1]), pk(acc[n][2], acc[n][3])};
        }
        lds_post(L, W_CB + w, 4);
        STAMP(8);
        {
            const int cc = lane & 3;
            unsigned sp = cc * HC_CH + cc * 32 + ((lane >> 2) & 3) * 136 + (t4 >> 4) * 8;
            int xo[8 - T1];
#pragma unroll
            for (int n = T1; n < 8; ++n) { xo[n - T1] = OFF_X2 + b * 32768 + ((n * 4096 + t4 * 16 + b * 128) & 32767); PIN(xo[n - T1]); }
            PIN(sp);
            lds_wait4<false>(L, W_CB, 4);
            v2u a[8 - T1];
#pragma unroll
            for (int n = T1; n < 8; ++n) a[n - T1] = *(const LAS v2u*)(L + sp + (n - T1) * 544);
#pragma unroll
            for (int n = T1; n < 8; ++n) __builtin_amdgcn_raw_buffer_store_b128((v4u){a[n - T1].x, a[n - T1].y, tl, th}, ws, xo[n - T1], 0, SC1);
            __builtin_amdgcn_sched_barrier(0);
        }
        STAMP(9);
        PIN(qpr);
        const float qp = lo(qpr);
        f32x2 cq[4][4], qn[4][4];
        const float wc[4] = {lo(wrr.x), hi(wrr.x), lo(wrr.y), hi(wrr.y)};
        float piv, sumw;
        {
            f32x2 s[3][4];
#pragma unroll
            for (int m = 0; m < 4; ++m) {
                const f32x2 pp = {rdl(qp, 2 * m), rdl(qp, 2 * m + 1)};
                cq[0][m] = (f32x2){lo(qr[2 * m].x), lo(qr[2 * m + 1].x)} - pp;
                cq[1][m] = (f32x2){hi(qr[2 * m].x), hi(qr[2 * m + 1].x)} - pp;
                cq[2][m] = (f32x2){lo(qr[2 * m].y), lo(qr[2 * m + 1].y)} - pp;
                cq[3][m] = (f32x2){hi(qr[2 * m].y), hi(qr[2 * m + 1].y)} - pp;
                s[0][m] = (cq[0][m] + cq[1][m]) + (cq[2][m] + cq[3][m]);
                s[1][m] = cq[0][m] * cq[0][m];
#pragma unroll
                for (int c = 1; c < 4; ++c) s[1][m] = __builtin_elementwise_fma(cq[c][m], cq[c][m], s[1][m]);
                const float sw = (wc[0] + wc[1]) + (wc[2] + wc[3]);
                s[2][m] = (f32x2){sw, sw};
            }
            row_partials<3>(L + L_RED, s, lane, w);
            lds_post(L, W_CB + w, 5);
            lds_wait4<false>(L, W_CB, 5);
            const LAS f32x4* rp = (const LAS f32x4*)(L + L_RED + (lane & 7) * 48);
            const float mean = sum4(rp[0]) * (1.f / DM);
            const float var = fmaxf(sum4(rp[1]) * (1.f / DM) - mean * mean, 0.f);
            sumw = sum4(rp[2]);
            piv = rb(qp + mean);
            const float dm = piv - qp, iv = inv_std(var);
#pragma unroll
            for (int m = 0; m < 4; ++m) {
                const f32x2 mm = {rdl(dm, 2 * m), rdl(dm, 2 * m + 1)}, ii = {rdl(iv, 2 * m), rdl(iv, 2 * m + 1)};
#pragma unroll
                for (int c = 0; c < 4; ++c) { cq[c][m] -= mm; qn[c][m] = cq[c][m] * ii; PIN(cq[c][m]); PIN(qn[c][m]); }
            }
        }
        STAMP(10);
        v4u y[8];
        {
            int off[8];
#pragma unroll
            for (int c = 0; c < 4; ++c) {
                const int P = (t4 >> 5) + 8 * ((t4 & 28) + c);
                off[2 * c] = OFF_X2 + P * 32768 + ((b + P) & 255) * 128 + (t4 & 3) * 16;
                off[2 * c + 1] = off[2 * c] + 64;
            }
            pollm<8>(ws, off, y, tag);
        }
        STAMP(11);
        f32x2 d[4][4];
        {
            f32x2 s[3][4];
#pragma unroll
            for (int m = 0; m < 4; ++m) {
#pragma unroll
                for (int c = 0; c < 4; ++c) { const unsigned yy = y[2 * c + (m >> 1)][m & 1]; d[c][m] = __builtin_elementwise_fma(qn[c][m], (f32x2){lo(yy), hi(yy)}, cq[c][m]); }
                s[0][m] = (d[0][m] + d[1][m]) + (d[2][m] + d[3][m]);
                s[1][m] = d[0][m] * d[0][m];
                s[2][m] = d[0][m] * wc[0];
#pragma unroll
                for (int c = 1; c < 4; ++c) { s[1][m] = __builtin_elementwise_fma(d[c][m], d[c][m], s[1][m]); s[2][m] = __builtin_elementwise_fma(d[c][m], (f32x2){wc[c], wc[c]}, s[2][m]); }
            }
            row_partials<3>(L + L_RED + 512, s, lane, w);
        }
        lds_post(L, W_CB + w, 6);
        lds_wait4<false>(L, W_CB, 6);
        float md, inv2, rr;
        {
            const LAS f32x4* rp = (const LAS f32x4*)(L + L_RED + 512 + (lane & 7) * 48);
            const float mean = sum4(rp[0]) * (1.f / DM);
            const float var = fmaxf(sum4(rp[1]) * (1.f / DM) - mean * mean, 0.f);
            md = rb(piv + mean) - piv;
            inv2 = inv_std(var);
            rr = (sum4(rp[2]) - md * sumw) * inv2;
            rr = rr == rr ? rr : -INFINITY;
        }
        float mx = fmaxf(rr, dppf<0x124>(rr)); mx = fmaxf(mx, dppf<0x122>(mx)); mx = fmaxf(mx, dppf<0x121>(mx));
        const int tb = __builtin_ctzll(__ballot(rr == mx)) & 7;
        if (tid == 0) __builtin_amdgcn_raw_buffer_store_b128((v4u){__float_as_uint(mx), (unsigned)(8 * b + tb), tl, th}, ws, OFF_CAND + b * 64, 0, SC1);
        v2u ocand, hcand;
        {
            float ds[4];
#pragma unroll
            for (int c = 0; c < 4; ++c) {
                const f32x8 dd = {d[c][0].x, d[c][0].y, d[c][1].x, d[c][1].y, d[c][2].x, d[c][2].y, d[c][3].x, d[c][3].y};
                ds[c] = dd[tb];
            }
            const float mdb = rdl(md, tb), ivb = rdl(inv2, tb), pvb = rdl(piv, tb);
            const unsigned n0 = pk((ds[0] - mdb) * ivb, (ds[1] - mdb) * ivb), n1 = pk((ds[2] - mdb) * ivb, (ds[3] - mdb) * ivb);
            __builtin_amdgcn_raw_buffer_store_b128((v4u){n0, n1, tl, th}, ws, OFF_ROWS + b * 4096 + t4 * 16, 0, SC1);
            hcand = (v2u){pk(ds[0] + pvb, ds[1] + pvb), pk(ds[2] + pvb, ds[3] + pvb)};
            ocand = (v2u){pk(lo(hcand.x) + lo(n0), hi(hcand.x) + hi(n0)), pk(lo(hcand.y) + lo(n1), hi(hcand.y) + hi(n1))};
        }
        __builtin_amdgcn_s_setprio(0);
        STAMP(12);
        const rsrc_t ro = mk(p.out, SEQ * DM * 2);
        v4u ov[4];
        {
#pragma unroll
            for (int m = 0; m < 4; ++m) {
                const float i0 = rdl(inv2, 2 * m), i1 = rdl(inv2, 2 * m + 1);
                const f32x2 sc = {1.f + i0, 1.f + i1};
                const f32x2 of = {rdl(piv, 2 * m) - rdl(md, 2 * m) * i0, rdl(piv, 2 * m + 1) - rdl(md, 2 * m + 1) * i1};
                f32x2 o[4];
#pragma unroll
                for (int c = 0; c < 4; ++c) o[c] = __builtin_elementwise_fma(d[c][m], sc, of);
                const unsigned a0 = pk(o[0].x, o[1].x), a1 = pk(o[2].x, o[3].x), c0 = pk(o[0].y, o[1].y), c1 = pk(o[2].y, o[3].y);
                const bool odd = lane & 1;
                const unsigned e0 = dppu<0xB1>(odd ? a0 : c0), e1 = dppu<0xB1>(odd ? a1 : c1);
                ov[m] = odd ? (v4u){e0, e1, c0, c1} : (v4u){a0, a1, e0, e1};
            }
        }
#pragma unroll
        for (int m = 0; m < 4; ++m) { const int t = 2 * m + (lane & 1); __builtin_amdgcn_raw_buffer_store_b128(ov[m], ro, t != tb ? (8 * b + t) * 2048 + (t4 >> 1) * 16 : OOB, 0, OUT_AUX); }
        lds_wait4<true>(L, W_WIN, 1);
        int win;
        {
            const v4u wi = *(volatile LAS v4u*)(L + L_SYNC + 4 * W_WIN);
            const f32x4 wr4 = *(volatile LAS f32x4*)(L + L_SYNC + 4 * W_WR);
            float rw = wr4[0]; unsigned bi = wi.x;
            if (wr4[1] > rw) { rw = wr4[1]; bi = wi.y; }
            if (wr4[2] > rw) { rw = wr4[2]; bi = wi.z; }
            if (wr4[3] > rw) { rw = wr4[3]; bi = wi.w; }
            win = __builtin_amdgcn_readfirstlane((bi - 1) & 2047);
        }
        if (win != 8 * b + tb) __builtin_amdgcn_raw_buffer_store_b64(ocand, ro, (8 * b + tb) * 2048 + t4 * 8, 0, OUT_AUX);
        else __builtin_amdgcn_raw_buffer_store_b128((v4u){hcand.x, hcand.y, tl, th}, ws, OFF_H + t4 * 16, 0, SC1);
        STAMP(13);
    } else {
        if (wave == 4) *(LAS unsigned*)(L + L_SYNC + lane * 4) = 0u;
        unsigned junk = 0, junk2 = 0;
        {
            const rsrc_t rc = mk((const void*)(pc0 & ~255ull), CODE_BYTES);
            if (w < 3) junk = __builtin_amdgcn_raw_buffer_load_b32(rc, t4 * 128, 0, 0);
        }
        asm volatile("s_waitcnt lgkmcnt(0)" ::: "memory");
        __builtin_amdgcn_s_barrier();
        CFENCE();
        __builtin_amdgcn_sched_barrier(0);
        STAMP(1);
        const rsrc_t ws = mk(p.ws, WS_USED);
        v2u ep = {0u, 0u};
        if (w == 0) ep = __builtin_amdgcn_raw_buffer_load_b64(ws, OFF_EPOCH, 0, SC1);
        const int fs = j < 28 ? 28 * x + j : -1;
        const rsrc_t rh = mk(p.hk + chc * SEQ, SEQ * 2);
        v4u ha[5], hb[5];
#pragma unroll
        for (int i = 0; i < 5; ++i) {
            const int c = lane + 64 * i;
            hb[i] = __builtin_amdgcn_raw_buffer_load_b128(rh, c >= 1 ? c * 16 - 16 : OOB, 0, H_AUX);
        }
        v4u wt[24];
        const int qd = fs >> 2, sq = fs & 3;
        const unsigned wbytes = fs >= 0 ? DM * FF * 2 : 0;
        const rsrc_t rg_ = mk(p.wg, wbytes), ru_ = mk(p.wu, wbytes), rd_ = mk(p.wd, wbytes);
        const int go = (256 * sq + 64 * w + (lane >> 3)) * (FF * 2) + qd * 128 + (lane & 7) * 16;
        const int dno = (64 * qd + (lane >> 3)) * 2048 + sq * 512 + w * 128 + (lane & 7) * 16;
#define WISSUE(a, e) do { _Pragma("unroll") for (int i = (a); i < (e); ++i) { \
            if (i < 16) wt[i] = __builtin_amdgcn_raw_buffer_load_b128(i < 8 ? rg_ : ru_, go, (i & 7) * 8 * FF * 2, NT); \
            else wt[i] = __builtin_amdgcn_raw_buffer_load_b128(rd_, dno, (i - 16) * 8 * 2048, NT); } \
            __builtin_amdgcn_sched_barrier(0); } while (0)
#define VMW(n) do { asm volatile("s_waitcnt vmcnt(%0)" :: "n"(n) : "memory"); __builtin_amdgcn_sched_barrier(0); } while (0)
        __builtin_amdgcn_sched_barrier(0);
        WISSUE(0, NB0);
        if (w == 0) {
            const u64 t = (((u64)ep.y << 32) | ep.x) + 1;
            CFENCE();
            *(volatile LAS v4u*)(L + L_SYNC + 4 * W_TAG) = (v4u){(unsigned)t, (unsigned)(t >> 32), 1u, 0u};
            CFENCE();
        }
        STAMP(2);
        {
            LAS unsigned char* hp = L + w * HC_CH + lane * 16;
#pragma unroll
            for (int i = 0; i < 5; ++i) {
#pragma unroll
                for (int c = 0; c < 4; ++c)
                    ha[i][c] = __builtin_amdgcn_update_dpp(i ? __builtin_amdgcn_readlane(hb[i ? i - 1 : 0][c], 63) : 0u, hb[i][c], 0x138, 0xF, 0xF, false);
                const unsigned e[8] = {ha[i].x, ha[i].y, ha[i].z, ha[i].w, hb[i].x, hb[i].y, hb[i].z, hb[i].w};
                unsigned o[7];
#pragma unroll
                for (int jj = 0; jj < 7; ++jj) o[jj] = __builtin_amdgcn_alignbit(e[jj + 1], e[jj], 16);
                if (i < 4 || lane < 4) {
#pragma unroll
                    for (int a = 0; a < 4; ++a) {
                        *(LAS v4u*)(hp + (2 * a) * HC_COPY + i * 1024) = (v4u){o[a], o[a + 1], o[a + 2], o[a + 3]};
                        *(LAS v4u*)(hp + (2 * a + 1) * HC_COPY + i * 1024) = (v4u){e[a + 1], e[a + 2], e[a + 3], e[a + 4]};
                    }
                }
            }
        }
        const u64 tag = lds_tag(L);
        const unsigned tl = (unsigned)tag, th = (unsigned)(tag >> 32);
        lds_post(L, W_HC + w, 1);
        asm volatile("" :: "v"(junk), "v"(junk2));
        STAMP(3);
        if (NB1 > NB0) { lds_wait1<true>(L, W_CB + w, 1); WISSUE(NB0, NB1); }
        lds_wait4<true>(L, W_XI, 1);
        WISSUE(NB1, NB2);
        STAMP(4);
        lds_wait4<true>(L, W_CB, 3);
        {
            const LAS unsigned char* sp = L + L_STG + (t4 >> 4) * 128 + (((t4 & 15) * 8 + (t4 >> 4) * 8) & 127);
#pragma unroll
            for (int n = 0; n < T1; ++n) {
                const v2u a = *(const LAS v2u*)(sp + n * 2048);
                __builtin_amdgcn_raw_buffer_store_b128((v4u){a.x, a.y, tl, th}, ws, OFF_X2 + b * 32768 + ((n * 4096 + t4 * 16 + b * 128) & 32767), 0, SC1);
            }
        }
        __builtin_amdgcn_sched_barrier(0);
        STAMP(12);
        WISSUE(NB2, NB3);
        STAMP(5);
        lds_wait4<true>(L, W_CB, 4);
        WISSUE(NB3, NB4);
        if (NB4 < 24) { lds_wait4<true>(L, W_CB, 6); WISSUE(NB4, 24); }
        if (fs >= 0) {
            LAS unsigned char* tp = L + L_TR + w * 5120 + (lane >> 3) * 160 + (lane & 7) * 16;
            const LAS unsigned char* tr = L + L_TR + w * 5120 + (lane >> 2) * 160 + (lane & 3) * 8;
#pragma unroll
            for (int k = 0; k < 24; k += 4) {
#pragma unroll
                for (int i = 0; i < 4; ++i) *(LAS v4u*)(tp + i * 1280) = wt[k + i];
                CFENCE();
#pragma unroll
                for (int i = 0; i < 4; ++i) {
                    const v2u a = __builtin_bit_cast(v2u, __builtin_amdgcn_ds_read_tr16_b64_v4i16((LAS s16x4*)(tr + i * 32)));
                    const v2u c = __builtin_bit_cast(v2u, __builtin_amdgcn_ds_read_tr16_b64_v4i16((LAS s16x4*)(tr + i * 32 + 2560)));
                    wt[k + i] = (v4u){a.x, a.y, c.x, c.y};
                }
                CFENCE();
            }
#pragma unroll
            for (int i = 0; i < 24; ++i) PIN(wt[i]);
        }
        STAMP(6);
        lds_wait4<true>(L, W_CB, 6);
        __builtin_amdgcn_s_setprio(3);
        STAMP(7);
        int win; float rw;
        {
            const int off[1] = {OFF_CAND + t4 * 64};
            v4u c[1];
            pollm<1>(ws, off, c, tag);
            const float br = __uint_as_float(c[0].x);
            const float mx = wave_max(br);
            const unsigned bw = __builtin_amdgcn_readlane(c[0].y, __builtin_ctzll(__ballot(br == mx)) & 63);
            lds_post(L, W_WR + w, __float_as_uint(mx));
            lds_post(L, W_WIN + w, (bw & 2047) + 1);
            lds_wait4<false>(L, W_WIN, 1);
            const v4u wi = *(volatile LAS v4u*)(L + L_SYNC + 4 * W_WIN);
            const f32x4 wr4 = *(volatile LAS f32x4*)(L + L_SYNC + 4 * W_WR);
            rw = wr4[0]; unsigned bi = wi.x;
            if (wr4[1] > rw) { rw = wr4[1]; bi = wi.y; }
            if (wr4[2] > rw) { rw = wr4[2]; bi = wi.z; }
            if (wr4[3] > rw) { rw = wr4[3]; bi = wi.w; }
            win = __builtin_amdgcn_readfirstlane((bi - 1) & 2047);
        }
        STAMP(8);
        const f32x4 z4 = {0.f, 0.f, 0.f, 0.f};
        if (fs >= 0) {
            const int g = lane >> 4, pc = 8 * ((lane >> 2) & 1) + 4 * ((lane >> 3) & 1) + g;
            const int un = 4 * pc + (lane & 3);
            v4u A0, A1;
            {
                const int off[1] = {OFF_ROWS + (win >> 3) * 4096 + (64 * sq + 16 * w + pc) * 16};
                v4u xs[1];
                pollg<1, false>(ws, off, xs, tag);
                STAMP(9);
                spread<0>(xs[0].x, xs[0].y, A0, A1);
            }
#pragma unroll
            for (int e = 0; e < 2; ++e) {
                float v[4];
#pragma unroll
                for (int c = 0; c < 4; ++c) v[c] = mfma(A1, wt[8 * e + 4 + c], mfma(A0, wt[8 * e + c], z4))[0];
                *(LAS float*)(L + L_GU + e * 1024 + lane * 16 + w * 4) = g < 2 ? (g ? v[1] : v[0]) : (g == 2 ? v[2] : v[3]);
            }
            lds_post(L, W_HB + w, 1);
            lds_wait4<false>(L, W_HB, 1);
            float gs = sum4(*(const LAS f32x4*)(L + L_GU + un * 16)), us = sum4(*(const LAS f32x4*)(L + L_GU + 1024 + un * 16));
            if (w == 0) __builtin_amdgcn_raw_buffer_store_b128((v4u){__float_as_uint(gs), __float_as_uint(us), tl, th}, ws, OFF_MID + fs * 1024 + lane * 16, 0, SC1);
            STAMP(10);
            {
                const int off[3] = {OFF_MID + (fs ^ 1) * 1024 + lane * 16, OFF_MID + (fs ^ 2) * 1024 + lane * 16, OFF_MID + (fs ^ 3) * 1024 + lane * 16};
                v4u m[3];
                pollg<3, false>(ws, off, m, tag);
                gs = (gs + __uint_as_float(m[0].x)) + (__uint_as_float(m[1].x) + __uint_as_float(m[2].x));
                us = (us + __uint_as_float(m[0].y)) + (__uint_as_float(m[1].y) + __uint_as_float(m[2].y));
            }
            STAMP(13);
            gs = rb(gs);
            const float a = rb(gs * __builtin_amdgcn_rcpf(1.f + __expf(-gs))) * rb(us);
            const unsigned ap = pk(a, dppf<0xB1>(a));
            spread<2>(ap, ap, A0, A1);
            unsigned f[4];
#pragma unroll
            for (int c = 0; c < 4; ++c) f[c] = __float_as_uint(mfma(A1, wt[20 + c], mfma(A0, wt[16 + c], z4))[0]);
            if (lane < 16)
                __builtin_amdgcn_raw_buffer_store_b128((v4u){__builtin_amdgcn_perm(f[0], f[3], 0x07060503), __builtin_amdgcn_perm(f[1], f[3], 0x07060502),
                                                             __builtin_amdgcn_perm(f[2], f[3], 0x07060501), tl},
                                                       ws, OFF_PART + fs * 1024 + w * 256 + lane * 16, 0, SC1);
            if (b == 0 && tid == 256) __builtin_amdgcn_raw_buffer_store_b64((v2u){tl, th}, ws, OFF_EPOCH, 0, SC1);
        } else if (w == 0) {
            const int rix = 4 * x + j - 28;
            const int col = 64 * (rix >> 1) + (lane & 48) + 8 * (rix & 1) + (lane & 7);
            float hn, hh;
            {
                const int off[2] = {OFF_ROWS + (win >> 3) * 4096 + (col >> 2) * 16, OFF_H + (col >> 2) * 16};
                v4u hx[2];
                pollg<2, false>(ws, off, hx, tag);
                const unsigned n2 = (lane & 2) ? hx[0].y : hx[0].x, h2 = (lane & 2) ? hx[1].y : hx[1].x;
                hn = (lane & 1) ? hi(n2) : lo(n2); hh = (lane & 1) ? hi(h2) : lo(h2);
            }
            float sg = rb(__builtin_amdgcn_rcpf(1.f + __expf(-rb(rw))));
            PIN(hn); PIN(hh); PIN(sg);
            STAMP(9);
            int off[7]; v4u pv[7];
#pragma unroll
            for (int i = 0; i < 7; ++i) off[i] = OFF_PART + (8 * i + (lane >> 3)) * 4096 + rix * 128 + (lane & 7) * 16;
            pollg<7, true>(ws, off, pv, tag);
            STAMP(10);
            float s[4] = {0.f, 0.f, 0.f, 0.f};
#pragma unroll
            for (int i = 0; i < 7; ++i) {
                s[0] += __uint_as_float(pv[i].x); s[1] += __uint_as_float(pv[i].y); s[2] += __uint_as_float(pv[i].z);
                s[3] += __uint_as_float(__builtin_amdgcn_perm(__builtin_amdgcn_perm(pv[i].x, pv[i].y, 0x04000c0c), pv[i].z, 0x0706000c));
            }
            float t = fold16(fold32(s[0], s[2]), fold32(s[1], s[3]));
            t += dppf<0x128>(t);
            const float o = hh + rb(hn + rb(rb(t) * sg));
            const unsigned o2 = pk(o, dppf<0xB1>(o));
            if (!(lane & 9)) __builtin_amdgcn_raw_buffer_store_b32(o2, mk(p.out, SEQ * DM * 2), win * 2048 + col * 2, 0, SC1);
        }
        STAMP(11);
    }
}

extern "C" void kernel_launch(void* const* d_in, const int* in_sizes, int n_in, void* d_out, int out_size, void* d_ws, size_t ws_size,
                              hipStream_t stream) {
    Params p;
    p.q = (const u16*)d_in[0]; p.k = (const u16*)d_in[1]; p.v = (const u16*)d_in[2]; p.sk = (const u16*)d_in[3];
    p.hk = (const u16*)d_in[4]; p.dv = (const u16*)d_in[5]; p.wr = (const u16*)d_in[7]; p.wg = (const u16*)d_in[8];
    p.wu = (const u16*)d_in[9]; p.wd = (const u16*)d_in[10];
    p.out = (u16*)d_out; p.ws = (unsigned char*)d_ws + WS_BASE;
    (void)hipFuncSetAttribute((const void*)h3_mod_kernel, hipFuncAttributeMaxDynamicSharedMemorySize, LDS_BYTES);
    h3_mod_kernel<<<NWG, NTHR, LDS_BYTES, stream>>>(p);
}
```

```cpp
#define CODE_BYTES 25088
#include <hip/hip_runtime.h>
#include <hip/hip_bf16.h>

#define GAS __attribute__((address_space(1)))
#define LAS __attribute__((address_space(3)))
typedef unsigned short u16;
typedef unsigned long long u64;
typedef unsigned v4u __attribute__((ext_vector_type(4)));
typedef unsigned v3u __attribute__((ext_vector_type(3)));
typedef unsigned v2u __attribute__((ext_vector_type(2)));
typedef float f32x4 __attribute__((ext_vector_type(4)));
typedef float f32x2 __attribute__((ext_vector_type(2)));
typedef float f32x8 __attribute__((ext_vector_type(8)));
typedef __bf16 bf16x2 __attribute__((ext_vector_type(2)));
typedef __bf16 bf16x8 __attribute__((ext_vector_type(8)));
typedef short s16x4 __attribute__((ext_vector_type(4)));
typedef __amdgpu_buffer_rsrc_t rsrc_t;

constexpr int SEQ = 2048, DM = 1024, FF = 3584;
constexpr int NWG = 256, NTHR = 512;
constexpr int SC1 = 16;
constexpr int NT = 2;
constexpr int OOB = (int)0x80000000;

constexpr int OFF_EPOCH = 0;
#define HALF 7
constexpr int OFF_MID = 0;
constexpr int OFF_PART = 1 << 20;
constexpr int OFF_CAND = 448 << 10;
constexpr int OFF_G = 480 << 10;
constexpr int OFF_ROWS = 2 << 20;
constexpr int OFF_F1 = 4 << 20;
constexpr int OFF_X = 16 << 20;
constexpr int ALT = 0x82000, ALT_X = 0x802000;
template <int BIT> __device__ __forceinline__ constexpr int up(int o) { return (HALF & BIT) ? o + (o & ~511) + 512 : o + 1024; }
constexpr int WS_USED = 32 << 20;

#define NCP 8
#define CP 32
#define GP 16
#define GL 5
#define GC 17
#define GC2 19
#define GC3 25
#define GC4 28
#define OR2 (GC > 20 ? GC : 20)
#define CWR 4
#define QS 2
#define QR 2
#define D_KV 2
#define D_H 4
#define D_Y 16
#define D_CAND 10
#define D_C2 12
#define D_P1 2
#define D_POLL 30
#define D_PR 36
#define S_P3 3
#define D_F 10
#define FP2 6
#define YE 192
#define POLL_B 192
#define TR_EV 4
#define CAND_EV 7
#define CAND_EV2 5
#define OUTEV 3
#define XSH 1
#define HPRIO 3
#define HPRIO2 0
#define LATE_B 128
#define SLOW_B 128
#define NPQ 2
#define N1 12
#define TF 4
#define TS 2
#define T2 4
#define T3 (T2 - 2)
#define JW 1
#define DNOP 12
#define PX 0x2100
#define OUT_AUX SC1
#define WS_BASE 0

constexpr int HC_COPY = 260 * 16;
constexpr int HC_CH = 8 * HC_COPY;
constexpr int STG = 3328;
constexpr int XB_OFF = 4 * HC_CH;
constexpr int XB_CH = 286 * 16;
constexpr int L_SYNC = XB_OFF + 4 * XB_CH;
constexpr int L_ST = L_SYNC + 256;
constexpr int L_TR = L_ST + 1024;
constexpr int LDS_BYTES = L_TR + 4 * 2560;
constexpr int W_CB = 4, W_HB = 8, W_WIN = 12, W_WR = 16, W_EV = 20, W_XR = 24, W_YD = 28, W_Q = 32, W_IS = 36, W_OE = 40, W_K = 44, W_SW = 48, W_NEAR = 0, W_PL = 16, W_EA = 1;

constexpr unsigned SPIN_G = 1u << 18, SPIN_L = 1u << 22;

#define SB __builtin_amdgcn_sched_barrier(0)

struct Params {
    const u16 *q, *k, *v, *sk, *hk, *dv, *wr, *wg, *wu, *wd;
    u16* out;
    unsigned char *ws, *wx;
};

__device__ __forceinline__ rsrc_t mk(const void* p, unsigned bytes) { return __builtin_amdgcn_make_buffer_rsrc((void*)p, 0, bytes, 0x00020000); }
__device__ __forceinline__ float lo(unsigned x) { return __uint_as_float(x << 16); }
__device__ __forceinline__ float hi(unsigned x) { return __uint_as_float(x & 0xFFFF0000u); }
__device__ __forceinline__ unsigned pk(float a, float b) { f32x2 t = {a, b}; return __builtin_bit_cast(unsigned, __builtin_convertvector(t, bf16x2)); }
__device__ __forceinline__ float rb(float a) { return lo(pk(a, a) & 0xFFFFu); }
__device__ __forceinline__ f32x4 mfma(v4u a, v4u b, f32x4 c) {
    return __builtin_amdgcn_mfma_f32_16x16x32_bf16(__builtin_bit_cast(bf16x8, a), __builtin_bit_cast(bf16x8, b), c, 0, 0, 0);
}
template <int CTRL> __device__ __forceinline__ float dppf(float x) {
    return __uint_as_float(__builtin_amdgcn_update_dpp(0u, __float_as_uint(x), CTRL, 0xF, 0xF, true));
}
template <int CTRL> __device__ __forceinline__ unsigned dppu(unsigned x) { return __builtin_amdgcn_update_dpp(0u, x, CTRL, 0xF, 0xF, true); }
__device__ __forceinline__ float fold32(float a, float b) {
    auto r = __builtin_amdgcn_permlane32_swap(__float_as_uint(a), __float_as_uint(b), false, false);
    return __uint_as_float(r[0]) + __uint_as_float(r[1]);
}
__device__ __forceinline__ float fold16(float a, float b) {
    auto r = __builtin_amdgcn_permlane16_swap(__float_as_uint(a), __float_as_uint(b), false, false);
    return __uint_as_float(r[0]) + __uint_as_float(r[1]);
}
__device__ __forceinline__ unsigned wave_umax(unsigned x) {
    auto r = __builtin_amdgcn_permlane32_swap(x, x, false, false);
    x = max(r[0], r[1]);
    auto s = __builtin_amdgcn_permlane16_swap(x, x, false, false);
    x = max(s[0], s[1]);
    x = max(x, dppu<0x128>(x)); x = max(x, dppu<0x124>(x)); x = max(x, dppu<0x122>(x)); x = max(x, dppu<0x121>(x));
    return x;
}
__device__ __forceinline__ unsigned okey(float x) { const unsigned u = __float_as_uint(x + 0.f); return u ^ ((unsigned)((int)u >> 31) | 0x80000000u); }
__device__ __forceinline__ float unkey(unsigned k) { return __uint_as_float(k ^ (~(unsigned)((int)k >> 31) | 0x80000000u)); }
__device__ __forceinline__ u64 tagof(v4u x) { return ((u64)x.w << 32) | x.z; }

__device__ __forceinline__ u64 epoch(v2u e) { return (((u64)__builtin_amdgcn_readfirstlane(e.y) << 32) | __builtin_amdgcn_readfirstlane(e.x)) + 1; }
__device__ __forceinline__ u64 mktag(u64 e) { return e * 0x9E3779B97F4A7C15ull + 0x632BE59BD9B4E019ull; }
#define CFENCE() asm volatile("" ::: "memory")
#define PIN(x) asm volatile("" : "+v"(x))
__device__ __forceinline__ void lds_post(LAS unsigned char* L, int word, unsigned val) {
    CFENCE();
    *(volatile LAS unsigned*)(L + L_SYNC + 4 * word) = val;
    CFENCE();
}
template <bool SLEEP> __device__ __forceinline__ void lds_wait4(LAS unsigned char* L, int group, unsigned val) {
#pragma nounroll
    for (unsigned s = 0; s < SPIN_L; ++s) {
        const v4u x = *(volatile LAS v4u*)(L + L_SYNC + 4 * group);
        const unsigned m = __builtin_amdgcn_readfirstlane(min(min(x.x, x.y), min(x.z, x.w)));
        if (m >= val) break;
        if (SLEEP) __builtin_amdgcn_s_sleep(1);
    }
    CFENCE();
}
template <int N, bool T32> __device__ __forceinline__ unsigned pollg(rsrc_t rs, const int (&off)[N], v4u (&x)[N], u64 tag) {
    unsigned s = 0;
#pragma nounroll
    for (; s < SPIN_G; ++s) {
        bool bad = false;
#pragma unroll
        for (int i = 0; i < N; ++i) x[i] = __builtin_amdgcn_raw_buffer_load_b128(rs, off[i], 0, SC1);
#pragma unroll
        for (int i = 0; i < N; ++i) bad |= T32 ? x[i].w != (unsigned)tag : tagof(x[i]) != tag;
        if (!__builtin_amdgcn_ballot_w64(bad)) break;
        CFENCE();
    }
    CFENCE();
    return s;
}
template <int N> __device__ __forceinline__ bool missing(const v4u (&x)[N], u64 tag) {
    bool bad = false;
#pragma unroll
    for (int i = 0; i < N; ++i) bad |= tagof(x[i]) != tag;
    return __builtin_amdgcn_ballot_w64(bad) != 0;
}
template <int N, int SL = 0> __device__ __forceinline__ unsigned retry(rsrc_t rs, const int (&off)[N], int so, v4u (&x)[N], u64 tag) {
    unsigned s = 0;
    if (__builtin_expect(missing<N>(x, tag), 0)) {
#pragma nounroll
        while (++s < SPIN_G) {
            if (SL) __builtin_amdgcn_s_sleep(SL);
            CFENCE();
#pragma unroll
            for (int i = 0; i < N; ++i)
                if (tagof(x[i]) != tag) x[i] = __builtin_amdgcn_raw_buffer_load_b128(rs, off[i], so, SC1);
            if (!missing<N>(x, tag)) break;
        }
    }
    CFENCE();
    return s;
}
template <int D> __device__ __forceinline__ void spread(unsigned a, unsigned c, v4u& A0, v4u& A1) {
    A0 = (v4u){dppu<0x150>(a), dppu<0x150 + D>(c), dppu<0x158>(a), dppu<0x158 + D>(c)};
    A1 = (v4u){dppu<0x154>(a), dppu<0x154 + D>(c), dppu<0x15C>(a), dppu<0x15C + D>(c)};
}
__device__ __forceinline__ float sum4(f32x4 a) { return (a[0] + a[1]) + (a[2] + a[3]); }
__device__ __forceinline__ float inv_std(float var) { return __builtin_amdgcn_rcpf(rb(__builtin_amdgcn_sqrtf(rb(rb(var) + 1e-5f)))); }

__device__ __forceinline__ void swap32(v4u& a, v4u& c) {
#pragma unroll
    for (int e = 0; e < 4; ++e) { auto r = __builtin_amdgcn_permlane32_swap(a[e], c[e], false, false); a[e] = r[0]; c[e] = r[1]; }
}
#define DIAGLANE __builtin_amdgcn_inverse_ballot_w64(0xF0000F0000F0000Full)
__device__ __forceinline__ int cell(int pp, int l) {
    return OFF_X + 512 + (pp & 3) * 128 + ((l & 7) ^ ((pp >> 3) & 4)) * 1024 + (((pp >> 7) ^ (l >> 5)) & 1) * 8192 + (((pp >> 2) & 31) ^ ((pp >> 6) & 1)) * 16384 + (l >> 3) * 524288;
}
constexpr int P_L0 = 1024, P_L3 = 524288, P_J3 = 32768;
__device__ __forceinline__ int f1pos(int xc, int w, int j) { return OFF_F1 + up<2>(j * 64) + (w & 1) * 4096 + (xc >> 2) * 8192 + (w >> 1) * 16384 + (xc & 3) * 32768; }
__device__ __forceinline__ int candpos(int cp, int b) { return OFF_CAND + up<4>((b & 127) * CP) + (cp >> 2) * 8192 + (b >> 7) * 16384 + (cp & 3) * 524288; }
__device__ __forceinline__ int gpos(int xp, int w, int i) { return OFF_G + up<2>(((xp & 1) * 256 + w * 64 + i) * GP) + (xp >> 1) * 524288; }
__device__ __forceinline__ int partpos(int G, int o) { return OFF_PART + up<4>(o & 2047) + (G & 1) * 4096 + (o >> 11) * 8192 + (G >> 1) * 16384; }
__device__ __forceinline__ int alt(int o, unsigned m, int bit, int a) { return o ^ (__builtin_amdgcn_sbfe(m, bit, 1) & a); }

typedef u64 u64x8 __attribute__((ext_vector_type(8)));
typedef u64 u64x4 __attribute__((ext_vector_type(4)));
__global__ void __launch_bounds__(NTHR, 2) h3_mod_kernel(Params args) {
    extern __shared__ __attribute__((aligned(16))) unsigned char smem[];
    LAS unsigned char* L = (LAS unsigned char*)smem;
    u64 pc0, wxp;
    u64x8 ka;
    u64x4 kb;
    unsigned nr, pa, pb;
    asm volatile("s_load_dwordx2 %2, %6, 0x60\n\ts_load_dwordx16 %0, %6, 0x0\n\ts_load_dwordx8 %1, %6, 0x40\n\t"
                 "v_readfirstlane_b32 %3, %7\n\ts_cmp_lt_u32 %3, %11\n\ts_cbranch_scc0 1f\n\t"
                 "s_mul_i32 %3, %8, 0x8080\n\ts_and_b32 %3, %3, 0x3fc00\n\tv_lshl_or_b32 %4, %7, 19, %3\n\tv_xor_b32 %5, %10, %4\n\ts_mov_b64 exec, 15\n\ts_waitcnt lgkmcnt(0)\n\t"
                 "global_store_dword %4, %4, %2 sc1\n\tglobal_store_dword %5, %5, %2 sc1\n\ts_mov_b64 exec, -1\n\t"
                 "s_waitcnt vmcnt(1)\n\ts_nop %9\n\ts_getreg_b32 %3, hwreg(HW_REG_IB_STS, 0, 1)\n1:"
                 : "=&s"(ka), "=&s"(kb), "=&s"(wxp), "=&s"(nr), "=&v"(pa), "=&v"(pb)
                 : "s"(__builtin_amdgcn_kernarg_segment_ptr()), "v"(threadIdx.x), "s"(blockIdx.x), "n"(DNOP), "n"(PX), "n"(64) : "memory", "scc");
    asm volatile("s_getpc_b64 %0" : "=s"(pc0));
    SB;
    const int tid = threadIdx.x, lane = tid & 63;
    const int wave = __builtin_amdgcn_readfirstlane(tid >> 6), w = wave & 3;
    const int b = blockIdx.x, x = b & 7, j = b >> 3;
    const int t4 = 64 * w + lane;
    const int chc = 128 * x + 16 * (j >> 2) + (j & 3) + 4 * w;
    u64 tp = 0;
    unsigned tch = 0, toff = 0;
    {
        const unsigned slot = 4 * j + w, e = (slot & 15) > 12 ? (slot & 15) - 12 : slot & 15;
        toff = min(slot >> 4, (unsigned)(0x7236660030333ull >> (4 * e)) & 7) << (e >= 11 ? 21 : 20);
        asm volatile("s_load_dwordx2 %0, %1, %2" : "=&s"(tp) : "s"(__builtin_amdgcn_kernarg_segment_ptr()), "s"(e * 8));
    }
    unsigned junk = 0;
#define CODEPF() junk = __builtin_amdgcn_raw_buffer_load_b32(mk((const void*)(pc0 & ~255ull), wave == JW ? CODE_BYTES : 0), ((j & 3) * 64 + lane) * 128, 0, 0)
    const int xs = (x & 4) | ((x + XSH) & 3);
    const int cg = lane & 31;
    int vo = (512 * w + 16 * j + 8 * (lane >> 5)) * 2048 + xs * 256 + cg * 8;
    int vh = vo >= 3 * 2048 ? vo - 3 * 2048 : OOB;
    int sko = (32 * xs + cg) * 32;
    int fso = lane < 2 ? f1pos(xs, w, j) ^ (lane * ALT) : OOB;
    PIN(vo); PIN(vh); PIN(sko); PIN(fso);
    if (wave < 4) {
        if (lane < 48) *(LAS v4u*)(L + (lane < 32 ? XB_OFF + w * XB_CH + min(lane, 29) * 16 : w * HC_CH + ((lane >> 1) & 7) * HC_COPY + (lane & 1) * 16)) = (v4u){0u, 0u, 0u, 0u};
        if (w == 0) *(LAS unsigned*)(L + L_SYNC + lane * 4) = lane == W_NEAR ? (nr ^ (j >> 3) ^ (x >> 2) ^ 1) & 1 : 0u;
    } else { if (w >= 1) __builtin_amdgcn_s_sleep(D_KV); if (w >= 2) __builtin_amdgcn_s_sleep(D_KV); if (w >= 3) __builtin_amdgcn_s_sleep(D_KV); }
    SB;
    asm volatile("s_waitcnt lgkmcnt(0)" : "+s"(ka), "+s"(kb), "+s"(tp) : "s"(wxp) : "memory");
    Params p;
    p.q = (const u16*)ka[0]; p.k = (const u16*)ka[1]; p.v = (const u16*)ka[2]; p.sk = (const u16*)ka[3];
    p.hk = (const u16*)ka[4]; p.dv = (const u16*)ka[5]; p.wr = (const u16*)ka[6]; p.wg = (const u16*)ka[7];
    p.wu = (const u16*)kb[0]; p.wd = (const u16*)kb[1]; p.out = (u16*)kb[2]; p.ws = (unsigned char*)kb[3];
    u64 eps = *(const __attribute__((address_space(4))) u64*)p.ws;
    SB;
    const rsrc_t ws = mk(p.ws, WS_USED);
    const rsrc_t rk = mk(p.k, SEQ * DM * 2), rv = mk(p.v, SEQ * DM * 2);
    v2u kr[11], vr[8];
    v4u sa, sb;
    unsigned he[16];
    unsigned junk2 = 0, dbits;
#define HLOAD() do { const rsrc_t rh = mk(p.hk + chc * SEQ, SEQ * 2); \
        { _Pragma("unroll") for (int i = 0; i < 16; ++i) he[i] = __builtin_amdgcn_raw_buffer_load_b32(rh, lane * 4 + i * 256, 0, NT); } \
        SB; } while (0)
    if (__builtin_expect(wave >= 4, 1)) {
        asm volatile("s_load_dword %0, %1, %2" : "=&s"(tch) : "s"(tp), "s"(toff));
        sa = *(const GAS v4u*)((const GAS char*)p.sk + (unsigned)sko); sb = *(const GAS v4u*)((const GAS char*)p.sk + (unsigned)sko + 16);
#pragma unroll
        for (int i = 0; i < 3; ++i) kr[i] = __builtin_amdgcn_raw_buffer_load_b64(rk, vh, i * 2048, NT);
#pragma unroll
        for (int i = 0; i < 8; ++i) kr[3 + i] = __builtin_amdgcn_raw_buffer_load_b64(rk, vo, i * 2048, NT);
#pragma unroll
        for (int i = 0; i < 8; ++i) vr[i] = __builtin_amdgcn_raw_buffer_load_b64(rv, vo, i * 2048, NT);
        junk2 = __builtin_amdgcn_raw_buffer_load_b32(ws, fso, 0, SC1);
        SB;
        __builtin_amdgcn_s_barrier(); CFENCE(); SB;
    } else {
        CODEPF();
        __builtin_amdgcn_s_barrier(); CFENCE(); SB;
    }
    SB;
    int role = wave >> 2;
    asm volatile("" : "+s"(role));
    SB;
    if (role == 0) {
        const int t = lane & 3;
        int qo = (8 * b + t) * 2048 + w * 512 + ((lane >> 2) & 7) * 32 + (lane >> 5) * 16;
        v4u qr[4], wf[2];
        int yo[4];
        yo[0] = cell(32 * x + j, 32 * w + 4 * (lane >> 3)) + (lane & 7) * 16;
        PIN(yo[0]);
        dbits = __builtin_amdgcn_raw_buffer_load_b32(mk(p.dv, DM * 2), (chc * 2) & ~3, 0, 0);
        SB;
        __builtin_amdgcn_s_sleep(D_H);
        HLOAD();
        {
            const rsrc_t rw = mk(p.wr, DM * 2);
#pragma unroll
            for (int f = 0; f < 2; ++f) wf[f] = __builtin_amdgcn_raw_buffer_load_b128(rw, w * 512 + (lane >> 2) * 32 + f * 16, 0, 0);
            SB;
        }
        asm volatile("s_waitcnt lgkmcnt(0)" : "+s"(eps) :: "memory");
        const u64 epn = eps + 1;
        const u64 tag = mktag(epn);
        unsigned tl = (unsigned)tag, th = (unsigned)(tag >> 32);
        asm volatile("" : "+s"(tl), "+s"(th));
        {
            const unsigned m0v = (unsigned)(size_t)L + w * HC_CH;
            { const float d_ = (chc & 1) ? hi(dbits) : lo(dbits); const unsigned f_ = pk(lo(he[0]) + d_, hi(he[0])); if (lane == 0) he[0] = f_; }
#pragma unroll
            for (int i = 0; i < 16; ++i) {
                const unsigned pr = __builtin_amdgcn_update_dpp(i ? __builtin_amdgcn_readlane(he[i ? i - 1 : 0], 63) : 0u, he[i], 0x138, 0xF, 0xF, false);
                const unsigned o = __builtin_amdgcn_alignbit(he[i], pr, 16);
                asm volatile("s_mov_b32 m0, %2\n\ts_nop 0\n\t"
                             "ds_write_addtid_b32 %0 offset:%3\n\tds_write_addtid_b32 %0 offset:%4\n\tds_write_addtid_b32 %0 offset:%5\n\tds_write_addtid_b32 %0 offset:%6\n\t"
                             "ds_write_addtid_b32 %1 offset:%7\n\tds_write_addtid_b32 %1 offset:%8\n\tds_write_addtid_b32 %1 offset:%9\n\tds_write_addtid_b32 %1 offset:%10"
                             :: "v"(he[i]), "v"(o), "s"(m0v),
                                "n"(1 * HC_COPY + i * 256 + 28), "n"(3 * HC_COPY + i * 256 + 24), "n"(5 * HC_COPY + i * 256 + 20), "n"(7 * HC_COPY + i * 256 + 16),
                                "n"(0 * HC_COPY + i * 256 + 28), "n"(2 * HC_COPY + i * 256 + 24), "n"(4 * HC_COPY + i * 256 + 20), "n"(6 * HC_COPY + i * 256 + 16) : "memory", "m0");
            }
        }
        CFENCE();
        asm volatile("" :: "v"(junk));
        float sw;
        v4u wp[2];
        {
            const v4u one = {0x3F803F80u, 0x3F803F80u, 0x3F803F80u, 0x3F803F80u};
            f32x4 u = {0.f, 0.f, 0.f, 0.f};
#pragma unroll
            for (int f = 0; f < 2; ++f) {
                wp[f] = (v4u){__builtin_amdgcn_perm(wf[0][f + 2], wf[0][f], 0x05040100), __builtin_amdgcn_perm(wf[1][f + 2], wf[1][f], 0x05040100),
                              __builtin_amdgcn_perm(wf[0][f + 2], wf[0][f], 0x07060302), __builtin_amdgcn_perm(wf[1][f + 2], wf[1][f], 0x07060302)};
                if ((lane & 3) == 1) wp[f] = one;
                u = mfma(wp[f], one, u); PIN(wp[f]);
            }
            if (!(lane & 15)) *(LAS float*)(L + L_SYNC + 4 * W_SW + w * 16 + (lane >> 2)) = u[0];
        }
        v4u y[4];
        const rsrc_t ry = mk(p.ws, b < YE ? WS_USED : 0);
#define YL(n) do { y[n] = __builtin_amdgcn_raw_buffer_load_b128(ry, yo[n] + (n) * P_L0, 0, SC1); SB; } while (0)
        const v4u wsd = {(unsigned)(u64)p.ws, (unsigned)((u64)p.ws >> 32) & 0xFFFFu, (unsigned)WS_USED, 0x00020000u};
        unsigned pl;
#define YPLACE() do { const unsigned m = pl >> ((lane >> 1) & 28); \
                _Pragma("unroll") for (int n = 3; n >= 0; --n) { yo[n] = alt(yo[0], m, n, ALT_X); PIN(yo[n]); } } while (0)
        __builtin_amdgcn_s_setprio(2);
        unsigned sp[2];
        int xo;
        v4u cf;
        v2u oa[2];
        {
            const int i = lane & 15, g = lane >> 4;
            unsigned ap = w * HC_CH + (i & 7) * HC_COPY + (3 - g + (i >> 3)) * 16;
            unsigned bp = XB_OFF + w * XB_CH + (2 * i + g) * 16;
            unsigned st = w * HC_CH + STG + w * 64 + i * 32 + ((g ^ (i >> 2)) * 8);
            PIN(ap); PIN(bp); PIN(st);
            {
                const int cc = (lane >> 2) & 3;
#pragma unroll
                for (int n = 0; n < 2; ++n) {
                    sp[n] = cc * HC_CH + STG + cc * 64 + (lane >> 5) * HC_COPY + (4 * w + ((lane >> 3) & 2) + ((lane >> 1) & 1)) * 32 + ((2 * (lane & 1) + n) ^ w) * 8;
                    PIN(sp[n]);
                }
                xo = cell(32 * ((lane >> 2) & 7) + 4 * (lane >> 5) + w, 16 * x + ((j >> 1) & 12) + (j & 3)) + (j & 4) * 16 + t * 16;
                PIN(xo);
            }
#define CFLAG() do { CFENCE(); cf = *(volatile LAS v4u*)(L + L_SYNC + 4 * W_CB); SB; } while (0)
#define OREAD(s) do { if (__builtin_expect(__builtin_amdgcn_readfirstlane(min(min(cf.x, cf.y), min(cf.z, cf.w))) <= (s), 0)) lds_wait4<false>(L, W_CB, (s) + 1); \
                CFENCE(); \
                _Pragma("unroll") for (int n = 0; n < 2; ++n) oa[n] = __builtin_bit_cast(v2u, __builtin_amdgcn_ds_read_tr16_b64_v4i16((LAS s16x4*)(L + sp[n] + 2 * (s) * HC_COPY))); \
                SB; } while (0)
#define OPUT() do { __builtin_amdgcn_raw_buffer_store_b128((v4u){oa[0].x ^ tl, oa[0].y ^ th, oa[1].x, oa[1].y}, ws, xo, 0, SC1); xo += P_J3; \
                SB; } while (0)
            int go = gpos(x, w, lane), gb = 16 * (x & 1) + ((j >> 1) & 12) + (j & 3);
            PIN(go);
            asm volatile("" : "+s"(gb));
            v4u ot[3];
            unsigned gok = 0;
#define G_LD "buffer_load_dwordx3 v[252:254], %[go], %[rs], 0 offen sc1\n\t"
#define G_CMP "v_cmp_ne_u64_e32 vcc, %[tag], v[252:253]\n\t"
#define G_ST(k) "buffer_store_dwordx4 %[o" #k "], v255, %[rs], 0 offen sc1\n\t"
#define G_OK "v_bfe_i32 v255, v254, %[gb], 1\n\tv_and_b32 v255, %[ax], v255\n\tv_xor_b32 %[xo], v255, %[xo]\n\ts_mov_b32 %[ok], 1\n\t" \
             "buffer_store_dwordx4 %[o0], %[xo], %[rs], 0 offen sc1\n\tv_add_u32 v255, %[p1], %[xo]\n\t" G_ST(1)
#define G_T2 "v_add_u32 v255, %[p2], %[xo]\n\t" G_ST(2)
#define G_TRY "s_getreg_b32 %[t], hwreg(HW_REG_IB_STS, 0, 4)\n\ts_cmp_lg_u32 %[t], 0\n\ts_cbranch_scc1 3f\n\ts_waitcnt vmcnt(0)\n\t" G_CMP "s_cbranch_vccz 1f\n\t" G_LD "s_branch 3f\n1:\n\t" G_OK
#define G_IO(o2_) : [xo] "+v"(xo), [ok] "+s"(gok), [t] "=&s"(t_) : [go] "v"(go), [rs] "s"(wsd), [tag] "s"(tag), [gb] "s"(gb), [ax] "n"(ALT_X), [p1] "n"(P_J3), [p2] "n"(2 * P_J3), \
                  [o0] "v"(ot[0]), [o1] "v"(ot[1]), [o2] "v"(o2_), [sp] "n"(SPIN_G) : "memory", "scc", "vcc", "v252", "v253", "v254", "v255"
#define OTAKE(s) do { ot[s] = (v4u){oa[0].x ^ tl, oa[0].y ^ th, oa[1].x, oa[1].y}; PIN(ot[s]); SB; } while (0)
#define NSTEP(n) do { unsigned t_; \
                if ((n) == GL) asm volatile(G_LD :: [go] "v"(go), [rs] "s"(wsd) : "memory", "v252", "v253", "v254"); \
                if ((n) == 5) OTAKE(0); \
                if ((n) == GC) { OTAKE(1); asm volatile("s_waitcnt vmcnt(1)" : "+v"(qr[0]), "+v"(qr[1]), "+v"(qr[2]), "+v"(qr[3])); asm volatile(G_TRY "s_nop 1\n3:" G_IO(ot[1]));  } \
                if ((n) == GC2) asm volatile("s_cmp_lg_u32 %[ok], 0\n\ts_cbranch_scc1 3f\n\t" G_TRY "s_nop 1\n3:" G_IO(ot[1])); \
                if ((n) == OR2 - 2) CFLAG(); \
                if ((n) == OR2) OREAD(2); \
                if ((n) == OR2 + 2) { OTAKE(2); asm volatile("s_cmp_lg_u32 %[ok], 0\n\ts_cbranch_scc1 2f\n\t" G_TRY "2:\n\t" G_T2 "s_nop 1\n3:" G_IO(ot[2])); } \
                if ((n) == GC3 || (n) == GC4) asm volatile("s_cmp_lg_u32 %[ok], 0\n\ts_cbranch_scc1 3f\n\t" G_TRY G_T2 "s_nop 1\n3:" G_IO(ot[2])); \
                SB; } while (0)
#define NLAST() do { unsigned t_;  \
                asm volatile("s_cmp_lg_u32 %[ok], 0\n\ts_cbranch_scc1 3f\n\ts_mov_b32 %[t], %[sp]\n2:\n\ts_waitcnt vmcnt(0)\n\t" G_CMP "s_cbranch_vccz 1f\n\t" \
                             "s_sub_u32 %[t], %[t], 1\n\ts_cbranch_scc1 1f\n\t" G_LD "s_branch 2b\n1:\n\t" G_OK G_T2 "s_nop 1\n3:" G_IO(ot[2])); \
                xo += 3 * P_J3; PIN(xo); SB; } while (0)
#define LDA(m, r) (*(const LAS v4u*)(L + ap + 512 * (m) + 64 * (r)))
#define LDB(u, r) (*(const LAS v4u*)(L + bp + 512 * (u) + 448 - 64 * (r)))
#define MM(n, a, bq) do { asm volatile("v_mfma_f32_16x16x32_bf16 %0, %1, %2, %0" : "+v"(acc[n]) : "v"(a), "v"(bq)); SB; } while (0)
#define LD(d, s) do { d = s; SB; } while (0)
#define XFLAG(s) do { CFENCE(); xf = *(volatile LAS unsigned*)(L + L_SYNC + 4 * (W_XR + (s))); SB; } while (0)
#define XWAIT(s) do { if (__builtin_expect(!__builtin_amdgcn_readfirstlane(xf), 0)) { \
                _Pragma("nounroll") for (unsigned s_ = 0; s_ < SPIN_L; ++s_) { XFLAG(s); if (__builtin_amdgcn_readfirstlane(xf)) break; } } \
                CFENCE(); SB; } while (0)
#define STAGE2(s) do { _Pragma("unroll") for (int n = 2 * (s); n < 2 * (s) + 2; ++n) \
                *(LAS v2u*)(L + st + n * HC_COPY) = (v2u){pk(acc[n][0], acc[n][1]), pk(acc[n][2], acc[n][3])}; \
                lds_post(L, W_CB + w, (s) + 1); SB; } while (0)
            v4u A[8], R[2][8], B[2][2], C[8][2], E[8][2], F[8][2];
            f32x4 acc[8], qg[2] = {{0.f, 0.f, 0.f, 0.f}, {0.f, 0.f, 0.f, 0.f}}, qm[2] = {{0.f, 0.f, 0.f, 0.f}, {0.f, 0.f, 0.f, 0.f}};
            unsigned xf, qa;
#pragma unroll
            for (int m = 7; m >= 2; --m) A[m] = LDA(m, 0);
#pragma unroll
            for (int r = 0; r < 8; ++r) { R[0][r] = LDA(0, r); R[1][r] = LDA(1, r); }
#pragma unroll
            for (int n = 0; n < 8; ++n) { acc[n] = (f32x4){0.f, 0.f, 0.f, 0.f}; PIN(acc[n]); }
            SB;
            unsigned qdone = 0;
            unsigned qpa = (unsigned)(size_t)L + L_SYNC + 4 * (W_Q + w);
            v4u qsd = {(unsigned)(u64)p.q, (unsigned)((u64)p.q >> 32) & 0xFFFFu, (unsigned)(SEQ * DM * 2), 0x00020000u};
#define QASM(c) asm volatile("s_cmp_lg_u32 %[ok], 0\n\t" c " 1f\n\tbuffer_load_dwordx4 %0, %[o], %[rs], 0 offen nt\n\tbuffer_load_dwordx4 %1, %[o], %[rs], 0 offen offset:256 nt\n\t" \
                "buffer_load_dwordx4 %2, %[o], %[rs], %[s] offen nt\n\tbuffer_load_dwordx4 %3, %[o], %[rs], %[s] offen offset:256 nt\n\tds_write_b32 %[a], %[a]\n1:" \
                : "+v"(qr[0]), "+v"(qr[1]), "+v"(qr[2]), "+v"(qr[3]) : [ok] "s"(qdone), [o] "v"(qo), [rs] "s"(qsd), [s] "s"(8192), [a] "v"(qpa) : "memory", "scc")
            {
                PIN(qo); PIN(qpa); asm volatile("" : "+s"(qsd));
                asm volatile("" : "=v"(qr[0]), "=v"(qr[1]), "=v"(qr[2]), "=v"(qr[3]));
#pragma nounroll
                for (unsigned s_ = 0; s_ < SPIN_L; ++s_) {
                    const v4u ev = *(volatile LAS v4u*)(L + L_SYNC + 4 * W_EV);
                    XFLAG(0);
                    qdone = __builtin_amdgcn_readfirstlane(min(min(ev.x, ev.y), min(ev.z, ev.w))) >> 1;
                    if (qdone | __builtin_amdgcn_readfirstlane(xf)) break;
                }
                SB;
                QASM("s_cbranch_scc0");
                SB;
                XWAIT(0);
            }
            LD(B[0][0], LDB(0, 0)); LD(B[0][1], LDB(1, 0));
#pragma unroll
            for (int r = 0; r < 8; ++r) {
                const int c = r & 1;
                if (r < 7) { LD(B[c ^ 1][0], LDB(0, r + 1)); LD(B[c ^ 1][1], LDB(1, r + 1)); }
                if (r == 7) XFLAG(1);
#pragma unroll
                for (int m = 7; m >= 2; --m) {
                    MM(m, A[m], B[c][0]);
                    if (m < 7) MM(m + 1, A[m], B[c][1]);
                    if (r < 7) LD(A[m], LDA(m, r + 1)); else if (m < 6) LD(A[m], LDA(m, 0));
                    if (r == 7 && m == 4) { XWAIT(1); LD(B[0][0], LDB(2, 0)); LD(B[0][1], LDB(3, 0)); }
                }
                MM(1, R[1][r], B[c][0]); MM(2, R[1][r], B[c][1]);
                MM(0, R[0][r], B[c][0]); MM(1, R[0][r], B[c][1]);
                if (r == QR) { QASM("s_cbranch_scc1"); SB; }
                if (r == CWR) {
                    __builtin_amdgcn_raw_buffer_store_b128(R[0][7], ws, tid < NCP ? candpos(lane, b) : OOB, 0, SC1);
                    SB;
                }
            }
#pragma unroll
            for (int r = 0; r < 8; ++r) {
                const int c = r & 1;
                if (r < 7) { LD(B[c ^ 1][0], LDB(2, r + 1)); LD(B[c ^ 1][1], LDB(3, r + 1)); }
                if (r == 6) XFLAG(2);
                if (r == 7) XWAIT(2);
                if (r == 2) CFLAG();
                if (r == 3) OREAD(0);
                NSTEP(r);
#pragma unroll
                for (int m = 5; m >= 2; --m) {
                    MM(m + 2, A[m], B[c][0]);
                    if (m < 5) MM(m + 3, A[m], B[c][1]);
                    if (r < 7) LD(A[m], LDA(m, r + 1)); else { LD(C[5 - m][0], LDB(4, 5 - m)); LD(C[5 - m][1], LDB(5, 5 - m)); }
                    if (r == 0 && m == 4) STAGE2(0);
                }
                MM(3, R[1][r], B[c][0]); MM(4, R[1][r], B[c][1]);
                MM(2, R[0][r], B[c][0]); MM(3, R[0][r], B[c][1]);
            }
#pragma unroll
            for (int r = 0; r < 8; ++r) {
                NSTEP(r + 8);
                if (r == 4) CFLAG();
                if (r == 6) OREAD(1);
                MM(5, R[1][r], C[r][0]);
                if (r < 4) LD(C[r + 4][0], LDB(4, r + 4)); else if (r > 4) LD(E[r - 5][0], LDA(2, r - 5));
                MM(4, R[0][r], C[r][0]);
                if (r < 4) LD(C[r + 4][1], LDB(5, r + 4)); else if (r > 4) LD(E[r - 5][1], LDA(3, r - 5));
                MM(5, R[0][r], C[r][1]);
                if (r == 1) STAGE2(1);
            }
#pragma unroll
            for (int r = 0; r < 8; ++r) {
                if (r == 3) XFLAG(3);
                if (r == 5) XWAIT(3);
                NSTEP(r + 16);
                MM(7, E[r][1], C[r][0]);
                if (r < 5) LD(E[r + 3][0], LDA(2, r + 3)); else LD(F[r - 5][0], LDB(6, r - 5));
                MM(6, E[r][0], C[r][0]);
                if (r < 5) LD(E[r + 3][1], LDA(3, r + 3)); else LD(F[r - 5][1], LDB(7, r - 5));
                MM(7, E[r][0], C[r][1]);
                if (r == 7) LD(F[3][0], LDB(6, 3));
                MM(6, R[1][r], C[r][1]);
                if (r == 7) LD(F[3][1], LDB(7, 3));
                if (r >= QS && r < QS + 4) { const int k = (r - QS) >> 1; qg[k] = mfma(qr[r - QS], qr[r - QS], qg[k]); SB; qm[k] = mfma(wp[0], qr[r - QS], qm[k]); SB; }
                if (r == 0) STAGE2(2);
            }
#pragma unroll
            for (int r = 0; r < 8; ++r) {
                NSTEP(r + 24);
                MM(7, R[1][r], F[r][0]);
                if (r < 4) LD(F[r + 4][0], LDB(6, r + 4));
                if (r == 3) { qa = L_TR + 4096 + w * 512 + (lane & 15) * 16; PIN(qa); SB; }
                if (r == 4) {
                    if (DIAGLANE) {
                        *(LAS f32x4*)(L + qa) = qg[0]; *(LAS f32x4*)(L + qa + 256) = qg[1];
                        *(LAS float*)(L + qa + 2048) = qm[0][1]; *(LAS float*)(L + qa + 2052) = qm[1][1];
                    }
                    SB;
                }
                if (r == 7) { pl = *(const LAS unsigned*)(L + L_SYNC + 4 * (W_PL + w)); SB; }
                MM(6, R[0][r], F[r][0]);
                if (r < 4) LD(F[r + 4][1], LDB(7, r + 4));
                if (r == 5) { swap32(qr[0], qr[1]); SB; }
                if (r == 6) { swap32(qr[2], qr[3]); SB; }
                MM(7, R[0][r], F[r][1]);
            }
            asm volatile("s_nop 7");
            SB;
            STAGE2(3);
            NLAST();
        }
        CFLAG();
        {
#define OTR(s) do { _Pragma("unroll") for (int n = 0; n < 2; ++n) oa[n] = __builtin_bit_cast(v2u, __builtin_amdgcn_ds_read_tr16_b64_v4i16((LAS s16x4*)(L + sp[n] + 2 * (s) * HC_COPY))); } while (0)
            CFENCE(); OTR(3); CFENCE(); SB;
            YPLACE(); SB;
            if (__builtin_expect(__builtin_amdgcn_readfirstlane(min(min(cf.x, cf.y), min(cf.z, cf.w))) <= 3, 0)) {
#pragma nounroll
                for (unsigned s_ = 0; s_ < SPIN_L; ++s_) {
                    CFLAG(); CFENCE(); OTR(3); CFENCE();
                    if (__builtin_amdgcn_readfirstlane(min(min(cf.x, cf.y), min(cf.z, cf.w))) > 3) break;
                }
            }
            SB;
        }
        OPUT();
        lds_post(L, W_YD + w, 1);
        f32x2 d0[2][4][2], d[2][4][2], qn[2][4][2];
#define QSUM() do { { const LAS unsigned char* q_ = L + L_TR + 4096 + (lane & 15) * 16; f32x2 g_[4], m_[4]; \
                _Pragma("unroll") for (int i = 0; i < 4; ++i) { g_[i] = (f32x2){*(const LAS float*)(q_ + t * 4 + i * 512), *(const LAS float*)(q_ + t * 4 + i * 512 + 256)}; m_[i] = *(const LAS f32x2*)(q_ + 2048 + i * 512); } \
                const f32x2 gg = (g_[0] + g_[1]) + (g_[2] + g_[3]), mm = (m_[0] + m_[1]) + (m_[2] + m_[3]); \
                st = (f32x4){gg.x, mm.x, gg.y, mm.y}; \
                _Pragma("unroll") for (int e = 0; e < 4; ++e) { st[e] += dppf<0x128>(st[e]); st[e] += dppf<0x124>(st[e]); } \
            } } while (0)
        float piv[2];
        v2u old[4];
        {
            f32x4 st;
            {
            QSUM();
            sw = sum4(*(const LAS f32x4*)(L + L_SYNC + 4 * W_SW + t * 16));
            sw += dppf<0x4E>(sw); sw += dppf<0xB1>(sw);
            PIN(st);
            PIN(sw);
            {
            lds_wait4<false>(L, W_YD, 1);
#pragma unroll
            for (int n = 0; n < 4; ++n) YL(n);
            }
            }
            lds_post(L, W_CB + w, 5);
#pragma unroll
            for (int n = 0; n < 4; ++n) old[n] = *(const LAS v2u*)(L + XB_OFF + t * XB_CH + (30 + 64 * w + (lane & 56) + ((lane >> 2) & 1) + 2 * n) * 16);
#pragma unroll
            for (int k = 0; k < 2; ++k) {
                const float mean = st[2 * k + 1] * (1.f / DM);
                const float iv = inv_std(fmaxf(st[2 * k] * (1.f / DM) - mean * mean, 0.f));
                piv[k] = rb(mean);
#pragma unroll
                for (int a = 0; a < 4; ++a)
#pragma unroll
                    for (int h = 0; h < 2; ++h) {
                        const unsigned q0 = qr[2 * k + h][a >> 1], q1 = qr[2 * k + h][(a >> 1) + 2];
                        d0[k][a][h] = ((a & 1) ? (f32x2){hi(q0), hi(q1)} : (f32x2){lo(q0), lo(q1)}) - (f32x2){piv[k], piv[k]};
                        qn[k][a][h] = d0[k][a][h] * iv;
                        PIN(d0[k][a][h]); PIN(qn[k][a][h]);
                    }
            }
        }
        float pv1 = (lane & 16) ? piv[1] : piv[0];
        int rowk = 2047 - (8 * b + t + ((lane >> 2) & 4));
        int cs = tid < NCP ? candpos(lane, b) : OOB;
        unsigned rw_ = L_TR + w * 512 + (lane & 15) * 16, rw2 = L_TR + 2048 + w * 256 + (lane & 15) * 8, rr_ = L_TR + (lane & 31) * 16 + t * 4, rr2 = L_TR + 2048 + (lane & 31) * 8;
        PIN(pv1); PIN(rowk); PIN(cs); PIN(rw_); PIN(rw2); PIN(rr_); PIN(rr2);
        {
            if (b >= YE) __builtin_amdgcn_s_sleep(D_Y);
            if (b >= YE) {
#pragma unroll
                for (int n = 0; n < 4; ++n) y[n] = __builtin_amdgcn_raw_buffer_load_b128(ws, yo[n] + n * P_L0, 0, SC1);
            }
            unsigned s = 0;
            f32x4 g[2], sm[2];
            v4u pd[2];
#define YOLD(a) __builtin_amdgcn_ballot_w64((((u64)y[a].y << 32) | y[a].x) == (((u64)old[a].y << 32) | old[a].x))
#pragma nounroll
            for (;;) {
                u64 miss[4];
#pragma unroll
                for (int a = 0; a < 4; ++a) {
                    miss[a] = YOLD(a);
                    if (a == 3) SB;
#pragma unroll
                    for (int k = 0; k < 2; ++k)
#pragma unroll
                        for (int h = 0; h < 2; ++h) {
                            const unsigned yy = y[a][2 * k + h] ^ (k ? 0u : h ? th : tl);
                            d[k][a][h] = __builtin_elementwise_fma(qn[k][a][h], (f32x2){lo(yy), hi(yy)}, d0[k][a][h]);
                            pd[k][2 * (a & 1) + h] = pk(d[k][a][h].x, d[k][a][h].y);
                        }
                    if (a & 1) {
                        const f32x4 z = {0.f, 0.f, 0.f, 0.f};
#pragma unroll
                        for (int k = 0; k < 2; ++k) { g[k] = mfma(pd[k], pd[k], a > 1 ? g[k] : z); sm[k] = mfma(wp[a >> 1], pd[k], a > 1 ? sm[k] : z); }
                    }
                    if (a == 2) { PIN(pd[0]); PIN(pd[1]); }
                    SB;
                }
                if (__builtin_expect(!(miss[0] | miss[1] | miss[2] | miss[3]), 1) || s) break;
#pragma nounroll
                do {
#pragma unroll
                    for (int n = 0; n < 4; ++n)
                        if (__builtin_amdgcn_inverse_ballot_w64(miss[n])) y[n] = __builtin_amdgcn_raw_buffer_load_b128(ws, yo[n] + n * P_L0, 0, SC1);
#pragma unroll
                    for (int n = 0; n < 4; ++n) miss[n] = YOLD(n);
                } while (++s < SPIN_G && (miss[0] | miss[1] | miss[2] | miss[3]));
                SB;
            }
            if (DIAGLANE) {
#pragma unroll
                for (int k = 0; k < 2; ++k) { *(LAS f32x4*)(L + rw_ + k * 256) = g[k]; *(LAS f32x2*)(L + rw2 + k * 128) = (f32x2){sm[k][0], sm[k][1]}; }
            }
        }
        lds_post(L, W_CB + w, 6);
        float md1, inv1, rr;
        {
            float a[4];
            f32x2 c[4];
#define RSUM() ({ const v4u f = *(volatile LAS v4u*)(L + L_SYNC + 4 * W_CB); CFENCE(); \
                _Pragma("unroll") for (int i = 0; i < 4; ++i) { a[i] = *(const LAS float*)(L + rr_ + i * 512); c[i] = *(const LAS f32x2*)(L + rr2 + i * 256); } \
                CFENCE(); __builtin_amdgcn_readfirstlane(min(min(f.x, f.y), min(f.z, f.w))) >= 6; })
            {
#pragma nounroll
                for (unsigned s = 0; s < SPIN_L; ++s) if (RSUM()) break;
            }
            const f32x2 cc = (c[0] + c[1]) + (c[2] + c[3]);
            const f32x2 aa = (f32x2){a[0], a[1]} + (f32x2){a[2], a[3]};
            float s[3] = {aa.x + aa.y, cc.y, cc.x};
#pragma unroll
            for (int e = 0; e < 3; ++e) { s[e] += dppf<0x128>(s[e]); asm("" : "+v"(s[e])); s[e] += dppf<0x124>(s[e]); asm("" : "+v"(s[e])); }
            const float mean = s[1] * (1.f / DM);
            const float var = fmaxf(s[0] * (1.f / DM) - mean * mean, 0.f);
            md1 = rb(pv1 + mean) - pv1;
            inv1 = inv_std(var);
            rr = (s[2] - md1 * sw) * inv1;
            rr = rr == rr ? rr : -INFINITY;
        }
        unsigned mx = (okey(rr) & ~2047u) | rowk;
        mx = max(mx, dppu<0x122>(mx)); mx = max(mx, dppu<0x121>(mx));
        { auto r = __builtin_amdgcn_permlane16_swap(mx, mx, false, false); mx = max(r[0], r[1]); }
        __builtin_amdgcn_raw_buffer_store_b128((v4u){mx, mx, tl, th}, ws, cs, 0, SC1);
        const int tb = ~__builtin_amdgcn_readfirstlane(mx) & 7;
        SB;
        float md[2], inv2[2];
        {
            auto r = __builtin_amdgcn_permlane16_swap(__float_as_uint(md1), __float_as_uint(md1), false, false);
            auto s = __builtin_amdgcn_permlane16_swap(__float_as_uint(inv1), __float_as_uint(inv1), false, false);
            md[0] = __uint_as_float(r[0]); md[1] = __uint_as_float(r[1]); inv2[0] = __uint_as_float(s[0]); inv2[1] = __uint_as_float(s[1]);
        }
        const int ro_ = t == (tb & 3) ? OFF_ROWS + b * 8192 + up<4>(w * 1024 + (lane >> 5) * 512 + ((lane >> 2) & 7) * 16) : OOB;
#define ROWPUT(k, o_, sc, of) do { f32x2 z[4][2]; \
                _Pragma("unroll") for (int a = 0; a < 4; ++a) _Pragma("unroll") for (int h = 0; h < 2; ++h) z[a][h] = __builtin_elementwise_fma(d[k][a][h], (f32x2){sc, sc}, (f32x2){of, of}); \
                _Pragma("unroll") for (int e = 0; e < 4; ++e) \
                    __builtin_amdgcn_raw_buffer_store_b128((v4u){pk(z[0][e >> 1][e & 1], z[1][e >> 1][e & 1]), pk(z[2][e >> 1][e & 1], z[3][e >> 1][e & 1]), tl, th}, ws, (o_) + e * 128, 0, SC1); } while (0)
        if (tb < 4) { ROWPUT(0, ro_, inv2[0], -md[0] * inv2[0]); SB; ROWPUT(0, ro_ - 512, 1.f, piv[0]); }
        else { ROWPUT(1, ro_, inv2[1], -md[1] * inv2[1]); SB; ROWPUT(1, ro_ - 512, 1.f, piv[1]); }
        __builtin_amdgcn_s_setprio(0);
        SB;
        const rsrc_t ro = mk(p.out, SEQ * DM * 2);
        v4u ov[4];
#pragma unroll
        for (int k = 0; k < 2; ++k) {
            const float sc = 1.f + inv2[k], of = piv[k] - md[k] * inv2[k];
            f32x2 z[4][2];
#pragma unroll
            for (int a = 0; a < 4; ++a)
#pragma unroll
                for (int h = 0; h < 2; ++h) z[a][h] = __builtin_elementwise_fma(d[k][a][h], (f32x2){sc, sc}, (f32x2){of, of});
#pragma unroll
            for (int h = 0; h < 2; ++h) ov[2 * k + h] = (v4u){pk(z[0][h].x, z[1][h].x), pk(z[2][h].x, z[3][h].x), pk(z[0][h].y, z[1][h].y), pk(z[2][h].y, z[3][h].y)};
            swap32(ov[2 * k], ov[2 * k + 1]);
        }
        lds_post(L, W_CB + w, 7);
#define OUTPUT(c0, c1) do { const int o0 = (c0) ? qo : OOB, o1 = (c1) ? qo + 8192 : OOB; \
            _Pragma("unroll") for (int f = 0; f < 4; ++f) __builtin_amdgcn_raw_buffer_store_b128(ov[f], ro, (f < 2 ? o0 : o1) + (f & 1) * 256, 0, OUT_AUX); } while (0)
        if (j >= 28) OUTPUT(t != tb, t + 4 != tb);
        if (w == 3) {
            if (b >= POLL_B) __builtin_amdgcn_s_sleep(D_CAND); else __builtin_amdgcn_s_sleep(D_C2);
            __builtin_amdgcn_s_setprio(3);
            const int off[1] = {candpos(x, t4) ^ (__builtin_popcount(pl) > 16 ? 8192 : 0)};
            v4u c[1];
            c[0] = __builtin_amdgcn_raw_buffer_load_b128(ws, off[0], 0, SC1);
            retry<1>(ws, off, 0, c, tag);
            lds_post(L, W_K + 3, wave_umax(c[0].x) | 0);
            __builtin_amdgcn_s_setprio(0);
        }
        bool done = j >= 28;
        lds_wait4<true>(L, W_K, 1);
        {
            const v4u kk = *(volatile LAS v4u*)(L + L_SYNC + 4 * W_K);
            const bool lost = mx != max(max(kk.x, kk.y), max(kk.z, kk.w));
            int o0 = (t == tb ? lost : !done) ? qo : OOB, o1 = (t + 4 == tb ? lost : !done) ? qo + 8192 : OOB;
            PIN(o0); PIN(o1);
            lds_wait4<true>(L, W_OE, OUTEV);
#pragma unroll
            for (int f = 0; f < 4; ++f) __builtin_amdgcn_raw_buffer_store_b128(ov[f], ro, (f < 2 ? o0 : o1) + (f & 1) * 256, 0, OUT_AUX);
        }
        if (b == 0 && tid == 0) { __builtin_amdgcn_raw_buffer_store_b64((v2u){(unsigned)epn, (unsigned)(epn >> 32)}, ws, OFF_EPOCH, 0, SC1); }
    } else {
        const int fs = j < 28 ? b : -1;
        v4u wt[24];
        const int c3 = (3 - (int)((u64)p.wg >> 7)) & 7, qn = (fs >> 2) - 7;
        const int qd = qn < 0 ? 8 * (fs >> 2) + c3 : 8 * (qn / 7) + ((qn % 7 + c3 + 1) & 7), sq = fs & 3;
        const unsigned wbytes = fs >= 0 ? DM * FF * 2 : 0;
        const rsrc_t rg_ = mk(p.wg, wbytes), ru_ = mk(p.wu, wbytes), rd_ = mk(p.wd, wbytes);
        int go = (256 * sq + 64 * w + (lane >> 3)) * (FF * 2) + qd * 128 + (lane & 7) * 16;
        const int dno = (64 * qd + (lane >> 3)) * 2048 + sq * 512 + w * 128 + (lane & 7) * 16;
#define WISSUE(a, e) do { _Pragma("unroll") for (int i_ = (a); i_ < (e); ++i_) { \
            if (i_ < 16) wt[i_] = __builtin_amdgcn_raw_buffer_load_b128(i_ < 8 ? rg_ : ru_, go, (i_ & 7) * 8 * FF * 2, NT); \
            else wt[i_] = __builtin_amdgcn_raw_buffer_load_b128(rd_, dno, (i_ - 16) * (8 * 2048), NT); } \
            SB; } while (0)
        __builtin_amdgcn_s_setprio(HPRIO);
        unsigned nearv = *(const LAS unsigned*)(L + L_SYNC + 4 * W_NEAR);
        asm volatile("s_waitcnt lgkmcnt(0)" : "+s"(eps), "+s"(tch) :: "memory");
        PIN(nearv);
        const u64 tag = mktag(eps + 1);
        unsigned tl = (unsigned)tag, th = (unsigned)(tag >> 32);
        asm volatile("" : "+s"(tl), "+s"(th));
        int so = alt(cell(32 * xs + (cg & 28), 32 * w + j) + (lane >> 5) * 64 + (cg & 3) * 16, nearv, 0, ALT_X);
        PIN(so);
        SB;
        {
            v4u kv[4];
            const f32x2 w01[4] = {{lo(sa.x), lo(sa.z)}, {hi(sa.x), hi(sa.z)}, {lo(sa.y), lo(sa.w)}, {hi(sa.y), hi(sa.w)}};
            const f32x2 w23[4] = {{lo(sb.x), lo(sb.z)}, {hi(sb.x), hi(sb.z)}, {lo(sb.y), lo(sb.w)}, {hi(sb.y), hi(sb.w)}};
            f32x2 ka[11], kb[11], xa[8], xb[8];
#pragma unroll
            for (int i = 0; i < 11; ++i) { ka[i] = (f32x2){lo(kr[i].x), hi(kr[i].x)}; kb[i] = (f32x2){lo(kr[i].y), hi(kr[i].y)}; }
#pragma unroll
            for (int t = 0; t < 8; ++t) {
                f32x2 a = ka[t] * w01[3], c = kb[t] * w23[3];
#pragma unroll
                for (int jj = 2; jj >= 0; --jj) { a = __builtin_elementwise_fma(ka[t + 3 - jj], w01[jj], a); c = __builtin_elementwise_fma(kb[t + 3 - jj], w23[jj], c); }
                xa[t] = a * (f32x2){lo(vr[t].x), hi(vr[t].x)};
                xb[t] = c * (f32x2){lo(vr[t].y), hi(vr[t].y)};
            }
#pragma unroll
            for (int m = 0; m < 4; ++m) {
                kv[0][m] = pk(xa[7 - 2 * m].x, xa[6 - 2 * m].x);
                kv[1][m] = pk(xa[7 - 2 * m].y, xa[6 - 2 * m].y);
                kv[2][m] = pk(xb[7 - 2 * m].x, xb[6 - 2 * m].x);
                kv[3][m] = pk(xb[7 - 2 * m].y, xb[6 - 2 * m].y);
            }
#pragma unroll
            for (int c = 0; c < 4; ++c) __builtin_amdgcn_raw_buffer_store_b128(kv[c], ws, so + c * 128, 0, SC1);
            SB;
        }
        lds_post(L, W_EV + w, 1);
        int go_ = cell(32 * x + j, 32 * w + (lane >> 3)) + (lane & 7) * 16;
        int gs_ = lane < 2 ? gpos(2 * w + lane, j & 3, ((j >> 2) & 3) + 4 * x + 2 * (j & 16)) : OOB;
        int fo = lane < 32 ? alt(f1pos(x, w, lane), nearv, 0, ALT) : OOB;
        unsigned tn = tl ^ nearv;
        PIN(go_); PIN(gs_); PIN(fo); PIN(tn);
        SB;
        {
        asm volatile("s_waitcnt vmcnt(0)" ::: "memory");
        __builtin_amdgcn_raw_buffer_store_b64((v2u){tn, th}, ws, fso, 0, SC1);
        }
        __builtin_amdgcn_s_sleep(D_F);
        u64 f0 = 0, f1 = 0, pl64 = 0;
        {
            const v4u wsd = {(unsigned)(u64)p.ws, (unsigned)((u64)p.ws >> 32) & 0xFFFFu, (unsigned)WS_USED, 0x00020000u};
            u64 m_, dn = 0xFFFFFFFF00000000ull;
            unsigned n_ = SPIN_G;
#define F_LD(f) "buffer_load_dwordx2 %[" #f "], %[fo], %[rs], 0 offen sc1\n\t"
#define F_CHK(f) "s_waitcnt vmcnt(%[d])\n\tv_cmp_eq_u64_e32 vcc, %[t0], %[" #f "]\n\tv_cmp_eq_u64_e64 %[m], %[t1], %[" #f "]\n\ts_or_b64 %[pl], %[pl], %[m]\n\ts_or_b64 vcc, vcc, %[m]\n\t" \
                 "s_or_b64 %[dn], %[dn], vcc\n\ts_cmp_eq_u64 %[dn], -1\n\ts_cbranch_scc1 2f\n\tv_cndmask_b32_e32 %[fo], %[fo], %[oob], vcc\n\t" F_LD(f)
#define F_PRE "s_waitcnt vmcnt(0)\n\t" F_CHK(f0)
#define F_IO : [f0] "=&v"(f0), [f1] "=&v"(f1), [fo] "+v"(fo), [pl] "+s"(pl64), [dn] "+s"(dn), [m] "=&s"(m_), [n] "+s"(n_) \
             : [rs] "s"(wsd), [t0] "s"(tag), [t1] "s"(tag ^ 1), [oob] "v"(OOB), [h] "n"(FP2), [d] "n"(1) : "memory", "scc", "vcc"
            asm volatile(F_LD(f0) F_PRE "s_sleep %[h]\n\t" F_LD(f1) "\n1:\n\t" F_CHK(f0) F_CHK(f1) "s_sub_u32 %[n], %[n], 1\n\ts_cbranch_scc0 1b\n2:" F_IO);
            SB;
        }
        unsigned plc;
        {
            v4u xx[4];
            plc = __builtin_amdgcn_readfirstlane((unsigned)pl64);
#pragma unroll
            for (int i = 0; i < 4; ++i) xx[i] = __builtin_amdgcn_raw_buffer_load_b128(ws, alt(go_, plc >> 8 * i, lane >> 3, ALT_X), i * P_L3, SC1);
            SB;
            lds_post(L, W_EV + w, 2);
            lds_post(L, W_PL + w, plc);
#pragma unroll
            for (int i = 0; i < 4; ++i) *(LAS v4u*)(L + XB_OFF + (lane & 3) * XB_CH + (30 + 64 * w + 16 * i + (lane >> 2)) * 16) = xx[i];
            lds_post(L, W_XR + w, 1);
            SB;
            asm volatile("s_waitcnt vmcnt(0)" :: "v"(f0), "v"(f1) : "memory");
            __builtin_amdgcn_raw_buffer_store_b96((v3u){tl, th, plc}, ws, gs_, 0, SC1);
            SB;
        }
        asm volatile("" :: "v"(junk2), "v"(junk));
        __builtin_amdgcn_s_setprio(HPRIO2);
        lds_wait4<true>(L, W_EV, 2);
        LAS unsigned char* tp = L + L_TR + w * 2560 + (lane >> 3) * 160 + (lane & 7) * 16;
        const LAS unsigned char* tr = L + L_TR + w * 2560 + (lane >> 2) * 160 + (lane & 3) * 8;
#define WTURN(k0, k1) do { \
            _Pragma("unroll") for (int k = (k0); k < (k1); k += 4) { v2u a[2][4]; \
                _Pragma("unroll") for (int h = 0; h < 2; ++h) { \
                    *(LAS v4u*)(tp) = wt[k + 2 * h]; *(LAS v4u*)(tp + 1280) = wt[k + 2 * h + 1]; \
                    CFENCE(); \
                    _Pragma("unroll") for (int i = 0; i < 4; ++i) a[h][i] = __builtin_bit_cast(v2u, __builtin_amdgcn_ds_read_tr16_b64_v4i16((LAS s16x4*)(tr + i * 32))); \
                    CFENCE(); } \
                _Pragma("unroll") for (int i = 0; i < 4; ++i) wt[k + i] = (v4u){a[0][i].x, a[0][i].y, a[1][i].x, a[1][i].y}; } \
            _Pragma("unroll") for (int i = (k0); i < (k1); ++i) PIN(wt[i]); SB; } while (0)
#define THR() do { asm volatile("s_waitcnt vmcnt(%0)" :: "n"(TF) : "memory"); if (b < SLOW_B) asm volatile("s_waitcnt vmcnt(%0)" :: "n"(TS) : "memory"); SB; } while (0)
#define WARM3() do { const int l1 = (lane & 15) * 16 + w * 256; \
            int o_ = alt((lane & 32) ? partpos(fs >> 2, sq * 1024 + l1) : OFF_MID + fs * 2048 + up<4>(l1), lane, 4, ALT); \
            if (fs < 0) o_ = OOB; \
            if (((lane >> 4) & 1) != (__builtin_popcount(plc) > 16)) o_ = OOB; \
            const v4u z_ = {~tl, ~tl, ~tl, ~tl}; \
            __builtin_amdgcn_raw_buffer_store_b128(z_, ws, o_, 0, SC1); \
            SB; } while (0)
        WARM3();
        WISSUE(0, NPQ);
        lds_wait4<true>(L, W_Q, 1);
#pragma unroll
        for (int i = NPQ; i < N1; i += 2) { if (i > TS) THR(); WISSUE(i, i + 2); }
        LAS unsigned char* gp = L + w * HC_CH + (lane >> 3) * 160 + (lane & 7) * 16;
#define WPUT(k0, k1) do { _Pragma("unroll") for (int k = (k0); k < (k1); ++k) *(LAS v4u*)(gp + (k >> 1) * HC_COPY + (k & 1) * 1280) = wt[k]; SB; } while (0)
        {
            constexpr int NA = N1;
#pragma unroll
            for (int i = N1; i < 24; i += 2) {
                if (b >= LATE_B) THR();
                else if (i == N1) {
                    lds_wait4<true>(L, W_CB, TR_EV);
                } else if (i >= N1 + T2) {
                    asm volatile("s_waitcnt vmcnt(%0)" :: "n"(T3) : "memory");
                }
                SB;
                WISSUE(i, i + 2);
                if ((i == N1 + T2 - 2) && b < LATE_B) {  WPUT(0, NA); }
            }
            if (b >= LATE_B) { lds_wait4<true>(L, W_CB, 4); WPUT(0, NA);  }
            WPUT(NA, 16);
        }
        asm volatile("s_waitcnt lgkmcnt(0)" ::: "memory");
        lds_post(L, W_HB + w, 1);
        lds_wait4<true>(L, W_HB, 1);
        {
            const LAS unsigned char* gr = L + (lane >> 2) * 160 + (lane & 3) * 8 + w * 32;
#pragma unroll
            for (int k = 0; k < 16; ++k) {
                v2u a[2];
#pragma unroll
                for (int h = 0; h < 2; ++h)
                    a[h] = __builtin_bit_cast(v2u, __builtin_amdgcn_ds_read_tr16_b64_v4i16((LAS s16x4*)(gr + ((k >> 1) & 3) * HC_CH + (4 * (k >> 3) + 2 * (k & 1) + h) * HC_COPY)));
                wt[k] = (v4u){a[0].x, a[0].y, a[1].x, a[1].y};
            }
#pragma unroll
            for (int k = 0; k < 16; ++k) PIN(wt[k]);
            SB;
        }
        const f32x4 z4 = {0.f, 0.f, 0.f, 0.f};
        const int g = lane >> 4, pc = 8 * ((lane >> 2) & 1) + 4 * ((lane >> 3) & 1) + g;
        const int maj = __builtin_popcount(plc) > 16 ? ALT : 0;
        int mo = (OFF_MID + (fs & ~3) * 2048 + up<4>((4 * pc + (lane & 3)) * 16)) ^ maj;
        int ms = lane < 32 ? alt(OFF_MID + fs * 2048 + up<4>((16 * w + (lane & 15)) * 16), lane, 4, ALT) : OOB;
        int xo_ = OFF_ROWS + up<4>(sq * 1024 + (lane & 4) * 128 + g * 128 + (lane & 3) * 32);
        int po = lane < 32 ? alt(partpos(fs >> 2, sq * 1024 + w * 256 + (lane & 15) * 16), lane, 4, ALT) : OOB;
        int co[1] = {candpos(x, t4) ^ (maj & 8192)};
        PIN(ms); PIN(mo); PIN(xo_); PIN(po); PIN(co[0]);
        SB;
        lds_wait4<true>(L, W_CB, b < POLL_B ? CAND_EV : CAND_EV2);
        if (b < POLL_B) { __builtin_amdgcn_s_sleep(D_P1); } else __builtin_amdgcn_s_sleep(D_POLL);
        if (j >= 28) __builtin_amdgcn_s_sleep(D_PR);
        __builtin_amdgcn_s_setprio(3);
        if (w < 3) {
            v4u c[1];
            c[0] = __builtin_amdgcn_raw_buffer_load_b128(ws, co[0], 0, SC1);
            retry<1, S_P3>(ws, co, 0, c, tag);
            lds_post(L, W_K + w, wave_umax(c[0].x));
        }
        unsigned K = 0;
#pragma nounroll
        for (unsigned s = 0; s < SPIN_L; ++s) {
            const v4u kk = *(volatile LAS v4u*)(L + L_SYNC + 4 * W_K);
            K = __builtin_amdgcn_readfirstlane(max(max(kk.x, kk.y), max(kk.z, kk.w)));
            if (__builtin_amdgcn_readfirstlane(min(min(kk.x, kk.y), kk.z))) break;
        }
        CFENCE();

#define LASTQ(refetch) do { unsigned last = 0; \
            _Pragma("nounroll") for (unsigned s_ = 0; s_ < SPIN_L; ++s_) { last = KLAST(); if (last) break; } \
            CFENCE(); \
            if (last > K) { K = last; refetch; } } while (0)
#define KLAST() __builtin_amdgcn_readfirstlane(*(volatile LAS unsigned*)(L + L_SYNC + 4 * (W_K + 3)))
        if (fs >= 0) {
            {
                v4u A0, A1;
                {
                    int off[2];
                    v4u xs[2];
#define XFETCH() do { off[0] = xo_ + (int)((~K & 2047) >> 3) * 8192; off[1] = off[0] + 16; \
                        _Pragma("unroll") for (int i_ = 0; i_ < 2; ++i_) xs[i_] = __builtin_amdgcn_raw_buffer_load_b128(ws, off[0] + 16 * i_, 0, SC1); } while (0)
                    XFETCH();
                    SB;
                    LASTQ(XFETCH());
                    lds_post(L, W_OE + w, 1);
                    retry<2>(ws, off, 0, xs, tag);
                    f32x4 c[4];
#pragma unroll
                    for (int q = 0; q < 4; ++q) {
                        const bool on = __builtin_amdgcn_inverse_ballot_w64(0x000F000F000F000Full << (4 * q));
                        const v4u a = {on ? xs[0].x : 0u, on ? xs[0].y : 0u, on ? xs[1].x : 0u, on ? xs[1].y : 0u};
#pragma unroll
                        for (int r = 0; r < 4; ++r) c[r] = mfma(a, wt[4 * q + r], q ? c[r] : z4);
                        if (q == 0) SB;
                    }
                    asm("" : "+v"(c[0]), "+v"(c[1]), "+v"(c[2]), "+v"(c[3]));
                    float v = c[0][0] + c[1][1];
                    asm("" : "+v"(v));
                    v = (v + c[2][2]) + c[3][3];
                    v = fold16(v, v);
                    auto u = __builtin_amdgcn_permlane32_swap(__float_as_uint(v), __float_as_uint(v), false, false);
                    __builtin_amdgcn_raw_buffer_store_b128((v4u){u[0], u[1], tl, th}, ws, ms, 0, SC1);
                }
                lds_post(L, W_OE + w, 2);
                lds_wait4<false>(L, W_CB, 7);
                WTURN(16, 24);
                v4u m[4];
                unsigned s = 0;
#pragma nounroll
                for (; s < SPIN_G; ++s) {
                    CFENCE();
#pragma unroll
                    for (int i = 0; i < 4; ++i) m[i] = __builtin_amdgcn_raw_buffer_load_b128(ws, mo, i * 2048, SC1);
                    if (!missing<4>(m, tag)) break;
                }
                {
                    const float gs = rb((__uint_as_float(m[0].x) + __uint_as_float(m[1].x)) + (__uint_as_float(m[2].x) + __uint_as_float(m[3].x)));
                    const float uu = (__uint_as_float(m[0].y) + __uint_as_float(m[1].y)) + (__uint_as_float(m[2].y) + __uint_as_float(m[3].y));
                    const float a = rb(gs * __builtin_amdgcn_rcpf(1.f + __expf(-gs))) * rb(uu);
                    const unsigned ap = pk(a, dppf<0xB1>(a));
                    spread<2>(ap, ap, A0, A1);
                    unsigned f[4];
#pragma unroll
                    for (int c_ = 0; c_ < 4; ++c_) f[c_] = __float_as_uint(mfma(A1, wt[20 + c_], mfma(A0, wt[16 + c_], z4))[0]);
                        __builtin_amdgcn_raw_buffer_store_b128((v4u){__builtin_amdgcn_perm(f[0], f[3], 0x07060503), __builtin_amdgcn_perm(f[1], f[3], 0x07060502),
                                                                     __builtin_amdgcn_perm(f[2], f[3], 0x07060501), tl},
                                                               ws, po, 0, SC1);
                }
            }
            lds_post(L, W_OE + w, 3);
        } else {
            lds_post(L, W_OE + w, 3);
            if (w == 0) {
                const int rix = 4 * x + j - 28;
                const int col = 64 * (rix >> 1) + (lane & 48) + 8 * (rix & 1) + (lane & 7);
                int off[7]; v4u pv[7], hx[2];
#pragma unroll
                for (int i = 0; i < 7; ++i) off[i] = partpos(8 * i + (lane >> 3), rix * 128 + (lane & 7) * 16) ^ maj;
                LASTQ((void)0);
                const int win = ~K & 2047;
                const int ro_ = OFF_ROWS + (win >> 3) * 8192 + up<4>((col >> 7) * 512 + ((col >> 2) & 3) * 128 + ((col >> 4) & 7) * 16);
                const int hof[2] = {ro_, ro_ - 512};
                pollg<2, false>(ws, hof, hx, tag);
                const unsigned n2 = (lane & 2) ? hx[0].y : hx[0].x, h2 = (lane & 2) ? hx[1].y : hx[1].x;
                float hn = (lane & 1) ? hi(n2) : lo(n2), hh = (lane & 1) ? hi(h2) : lo(h2);
                float sg = rb(__builtin_amdgcn_rcpf(1.f + __expf(-rb(unkey((K & ~2047u) | 1024u)))));
                PIN(hn); PIN(hh); PIN(sg);
                {
                    const unsigned tw = tl;
                    unsigned s = 0;
#pragma nounroll
                    for (; s < SPIN_G; ++s) {
                        bool bad = false;
#pragma unroll
                        for (int i = 0; i < 7; ++i) pv[i] = __builtin_amdgcn_raw_buffer_load_b128(ws, off[i], 0, SC1);
#pragma unroll
                        for (int i = 0; i < 7; ++i) bad |= pv[i].w != tw;
                        if (!__builtin_amdgcn_ballot_w64(bad)) break;
                        CFENCE();
                    }
                }
                {
                float s[4] = {0.f, 0.f, 0.f, 0.f};
#pragma unroll
                for (int i = 0; i < 7; ++i) {
                    s[0] += __uint_as_float(pv[i].x); s[1] += __uint_as_float(pv[i].y); s[2] += __uint_as_float(pv[i].z);
                    s[3] += __uint_as_float(__builtin_amdgcn_perm(__builtin_amdgcn_perm(pv[i].x, pv[i].y, 0x04000c0c), pv[i].z, 0x0706000c));
                }
                float t = fold16(fold32(s[0], s[2]), fold32(s[1], s[3]));
                t += dppf<0x128>(t);
                const float o = hh + rb(hn + rb(rb(t) * sg));
                const unsigned o2 = pk(o, dppf<0xB1>(o));
                if (!(lane & 9)) __builtin_amdgcn_raw_buffer_store_b32(o2, mk(p.out, SEQ * DM * 2), win * 2048 + col * 2, 0, SC1);
                }
            }
        }
    }
}

extern "C" void kernel_launch(void* const* d_in, const int* in_sizes, int n_in, void* d_out, int out_size, void* d_ws, size_t ws_size,
                              hipStream_t stream) {
    Params p;
    p.q = (const u16*)d_in[0]; p.k = (const u16*)d_in[1]; p.v = (const u16*)d_in[2]; p.sk = (const u16*)d_in[3];
    p.hk = (const u16*)d_in[4]; p.dv = (const u16*)d_in[5]; p.wr = (const u16*)d_in[7]; p.wg = (const u16*)d_in[8];
    p.wu = (const u16*)d_in[9]; p.wd = (const u16*)d_in[10];
    p.out = (u16*)d_out; p.ws = (unsigned char*)d_ws + WS_BASE; p.wx = p.ws + OFF_X;
    (void)hipFuncSetAttribute((const void*)h3_mod_kernel, hipFuncAttributeMaxDynamicSharedMemorySize, LDS_BYTES);
    h3_mod_kernel<<<NWG, NTHR, LDS_BYTES, stream>>>(p);
}
```
